```python
import torch

def _softmax(x, axis=-1):
    return torch.softmax(x, dim=axis)
def _gelu(x):
    return torch.nn.functional.gelu(x, approximate="tanh")
def _silu(x):
    return torch.nn.functional.silu(x)
def _sigmoid(x):
    return torch.sigmoid(x)
def _layernorm(x, axis=-1, eps=1e-5):
    m = x.mean(dim=axis, keepdim=True)
    v = x.var(dim=axis, keepdim=True, unbiased=False)
    return (x - m) / torch.sqrt(v + eps)
def _scatter_add(tgt, idx, src, axis=0):
    return tgt.clone().index_add_(axis, idx, src)
def _segment_sum(src, seg, n):
    out = torch.zeros((n,) + src.shape[1:], dtype=src.dtype)
    return out.index_add_(0, seg, src)
def _pad(x, pw):
    flat = tuple(v for pair in reversed(pw) for v in pair)
    return torch.nn.functional.pad(x, flat)

kernel_name = 'concat_heads__gqa_block_cached__mha_block_cached__near_ridge'

def setup_inputs():

    _w = (lambda a: (lambda t: t.to(torch.bfloat16) if t.is_floating_point() else t)(torch.as_tensor(a)))
    def _A_setup():
        B, Sq, D = 16, 32, 576
        Hq, Hkv, Sk, Dh = 32, 16, 32, 128
        assert Hq % Hkv == 0, (Hq, Hkv)
        return {
            "x": _w((lambda *s: torch.randn(*s, dtype=torch.bfloat16) * 0.8)(B, Sq, D)),
            "k_cache": _w((lambda *s: torch.randn(*s, dtype=torch.bfloat16) * 0.8)(B, Hkv, Sk, Dh)),
            "v_cache": _w((lambda *s: torch.randn(*s, dtype=torch.bfloat16) * 0.05)(B, Hkv, Sk, Dh)),
            "wq": _w((lambda *s: torch.randn(*s, dtype=torch.bfloat16) * 0.8)(D, Hq * Dh) * (D ** -0.5)),
            "wo": _w((lambda *s: torch.randn(*s, dtype=torch.bfloat16) * 0.05)(Hq * Dh, D) * ((Hq * Dh) ** -0.5)),
        }
    _A_in = _A_setup()

    def _B_setup():
        B, Sq, D = 16, 32, 576
        H, Sk, Dh = 32, 32, 128
        return {
            "x": _w((lambda *s: torch.randn(*s, dtype=torch.bfloat16) * 0.8)(B, Sq, D)),
            "k_cache": _w((lambda *s: torch.randn(*s, dtype=torch.bfloat16) * 0.8)(B, H, Sk, Dh)),
            "v_cache": _w((lambda *s: torch.randn(*s, dtype=torch.bfloat16) * 0.05)(B, H, Sk, Dh)),
            "wq": _w((lambda *s: torch.randn(*s, dtype=torch.bfloat16) * 0.8)(D, H * Dh) * (D ** -0.5)),
            "wo": _w((lambda *s: torch.randn(*s, dtype=torch.bfloat16) * 0.05)(H * Dh, D) * ((H * Dh) ** -0.5)),
        }
    _B_in = _B_setup()

    return {
        "A_x": _A_in["x"],
        "A_k_cache": _A_in["k_cache"],
        "A_v_cache": _A_in["v_cache"],
        "A_wq": _A_in["wq"],
        "A_wo": _A_in["wo"],
        "B_x": _B_in["x"],
        "B_k_cache": _B_in["k_cache"],
        "B_v_cache": _B_in["v_cache"],
        "B_wq": _B_in["wq"],
        "B_wo": _B_in["wo"],
    }

def reference(A_x, A_k_cache, A_v_cache, A_wq, A_wo, B_x, B_k_cache, B_v_cache, B_wq, B_wo):
    def _A_ref(x, k_cache, v_cache, wq, wo):
        B, Sq, D = x.shape
        Hq, Dh = 32, 128
        Hkv = k_cache.shape[1]
        rep = Hq // Hkv
        q = (lambda x, *d: x.permute(*d))((x @ wq).reshape(B, Sq, Hq, Dh), 0, 2, 1, 3)
        k = (lambda x, n, axis: torch.repeat_interleave(x, n, dim=axis))(k_cache, rep, axis=1)
        v = (lambda x, n, axis: torch.repeat_interleave(x, n, dim=axis))(v_cache, rep, axis=1)
        scores = torch.einsum("bhqd,bhkd->bhqk", q, k) * (Dh ** -0.5)
        p = _softmax(scores, axis=-1)
        o = torch.einsum("bhqk,bhkd->bhqd", p, v)
        return (lambda x, *d: x.permute(*d))(o, 0, 2, 1, 3).reshape(B, Sq, Hq * Dh) @ wo

    def _B_ref(x, k_cache, v_cache, wq, wo):
        B, Sq, D = x.shape
        H, Dh = 32, 128
        q = (lambda x, *d: x.permute(*d))((x @ wq).reshape(B, Sq, H, Dh), 0, 2, 1, 3)
        scores = torch.einsum("bhqd,bhkd->bhqk", q, k_cache) * (Dh ** -0.5)
        p = _softmax(scores, axis=-1)
        o = torch.einsum("bhqk,bhkd->bhqd", p, v_cache)
        return (lambda x, *d: x.permute(*d))(o, 0, 2, 1, 3).reshape(B, Sq, H * Dh) @ wo

    a = _A_ref(A_x, A_k_cache, A_v_cache, A_wq, A_wo)
    b = _B_ref(B_x, B_k_cache, B_v_cache, B_wq, B_wo)
    return torch.concatenate([a, b], axis=-1)
```

```cpp
#include <hip/hip_runtime.h>
#include <hip/hip_bf16.h>
typedef __hip_bfloat16 bf16;
static __device__ __forceinline__ float b2f(unsigned short u){ return __uint_as_float(((unsigned)u)<<16); }
static __device__ __forceinline__ unsigned short f2b(float f){ unsigned u=__float_as_uint(f); u += 0x7fffu + ((u>>16)&1u); return (unsigned short)(u>>16); }

__global__ void gemm_naive(const unsigned short* A, const unsigned short* B, unsigned short* C, int M, int N, int K, int ldc){
  __shared__ float As[16][17], Bs[16][17];
  int tx=threadIdx.x, ty=threadIdx.y;
  int row=blockIdx.y*16+ty, col=blockIdx.x*16+tx;
  float acc=0;
  for(int k0=0;k0<K;k0+=16){
    As[ty][tx]=b2f(A[(size_t)row*K+k0+tx]);
    Bs[ty][tx]=b2f(B[(size_t)(k0+ty)*N+col]);
    __syncthreads();
    for(int k=0;k<16;k++) acc+=As[ty][k]*Bs[k][tx];
    __syncthreads();
  }
  C[(size_t)row*ldc+col]=f2b(acc);
}
__global__ void attn_naive(const unsigned short* Q, const unsigned short* Kc, const unsigned short* Vc, unsigned short* O, int Hkv){
  int b=blockIdx.x/32, h=blockIdx.x%32;
  int hk = h/(32/Hkv);
  __shared__ float S[32][33];
  const unsigned short* q=Q+(size_t)b*32*4096+h*128;
  const unsigned short* k=Kc+((size_t)b*Hkv+hk)*32*128;
  const unsigned short* v=Vc+((size_t)b*Hkv+hk)*32*128;
  int t=threadIdx.x;
  for(int i=t;i<1024;i+=256){
    int r=i/32,c=i%32; float a=0;
    for(int d=0;d<128;d++) a+=b2f(q[r*4096+d])*b2f(k[c*128+d]);
    a=b2f(f2b(a));
    a=b2f(f2b(a*0.08838834764831845f));
    S[r][c]=a;
  }
  __syncthreads();
  if(t<32){
    float m=-1e30f; for(int c=0;c<32;c++) m=fmaxf(m,S[t][c]);
    float s=0; for(int c=0;c<32;c++){ float e=__expf(S[t][c]-m); S[t][c]=e; s+=e;}
    for(int c=0;c<32;c++) S[t][c]=b2f(f2b(S[t][c]/s));
  }
  __syncthreads();
  for(int i=t;i<4096;i+=256){
    int r=i/128,d=i%128; float a=0;
    for(int c=0;c<32;c++) a+=S[r][c]*b2f(v[c*128+d]);
    O[(size_t)(b*32+r)*4096+h*128+d]=f2b(a);
  }
}
extern "C" void kernel_launch(void* const* d_in, const int* in_sizes, int n_in,
                              void* d_out, int out_size, void* d_ws, size_t ws_size,
                              hipStream_t stream) {
  unsigned short* ws=(unsigned short*)d_ws;
  for(int br=0;br<2;br++){
    const unsigned short* x=(const unsigned short*)d_in[br*5+0];
    const unsigned short* kc=(const unsigned short*)d_in[br*5+1];
    const unsigned short* vc=(const unsigned short*)d_in[br*5+2];
    const unsigned short* wq=(const unsigned short*)d_in[br*5+3];
    const unsigned short* wo=(const unsigned short*)d_in[br*5+4];
    unsigned short* Q=ws+(size_t)br*2*512*4096;
    unsigned short* O=Q+512*4096;
    gemm_naive<<<dim3(4096/16,512/16),dim3(16,16),0,stream>>>(x,wq,Q,512,4096,576,4096);
    attn_naive<<<512,256,0,stream>>>(Q,kc,vc,O,br==0?16:32);
    gemm_naive<<<dim3(576/16,512/16),dim3(16,16),0,stream>>>(O,wo,(unsigned short*)d_out+br*576,512,576,4096,1152);
  }
}
```

```cpp
#include <hip/hip_runtime.h>
#include <stdint.h>

#define CONS_ASM \
  "v_mbcnt_lo_u32_b32 v240, -1, 0\n\t" \
  "v_mbcnt_hi_u32_b32 v240, -1, v240\n\t" \
  "v_mov_b32 v254, 0\n\t" \
  "buffer_load_dwordx2 v[176:177], v254, %[rw], %[sep] offen sc1\n\t" \
  "s_getpc_b64 s[90:91]\n\t" \
  ".Lfab_pc:\n\t" \
  "s_sub_u32 s90, s90, .Lfab_pc-fused_attn_blocks\n\t" \
  "s_subb_u32 s91, s91, 0\n\t" \
  "v_lshlrev_b32 v178, 14, v240\n\t" \
  "v_add_u32 v178, %[cpf], v178\n\t" \
  "v_min_u32 v178, .Lfab_end-fused_attn_blocks-4, v178\n\t" \
  "s_mov_b64 exec, 3\n\t" \
  "global_load_dword v178, v178, s[90:91]\n\t" \
  "s_mov_b64 exec, -1\n\t" \
  "v_lshlrev_b32 v168, 4, v240\n\t" \
  "v_and_b32 v241, 15, v240\n\t" \
  "v_lshrrev_b32 v242, 4, v240\n\t" \
  "v_bfe_u32 v243, v240, 2, 2\n\t" \
  "v_and_b32 v239, 3, v240\n\t" \
  "v_lshrrev_b32 v238, 1, v241\n\t" \
  "v_xor_b32 v238, v238, v242\n\t" \
  "v_lshlrev_b32 v238, 4, v238\n\t" \
  "v_lshl_add_u32 v238, v241, 7, v238\n\t" \
  "v_lshl_add_u32 v237, v242, 3, v243\n\t" \
  "v_lshlrev_b32 v237, 8, v237\n\t" \
  "v_and_b32 v236, 1, v242\n\t" \
  "v_lshl_add_u32 v236, v236, 2, v243\n\t" \
  "v_lshl_add_u32 v237, v236, 5, v237\n\t" \
  "v_lshl_add_u32 v237, v239, 3, v237\n\t" \
  "s_lshl_b32 %[t0], %[w], 12\n\t" \
  "v_add_u32 v244, %[t0], v238\n\t" \
  "v_xor_b32 v245, 64, v244\n\t" \
  "v_add_u32 v224, 16384, v237\n\t" \
  "v_xor_b32 v225, 32, v224\n\t" \
  "v_xor_b32 v226, 64, v224\n\t" \
  "v_xor_b32 v227, 96, v224\n\t" \
  "v_xor_b32 v228, 128, v224\n\t" \
  "v_xor_b32 v229, 160, v224\n\t" \
  "v_xor_b32 v230, 192, v224\n\t" \
  "v_xor_b32 v231, 224, v224\n\t" \
  "v_lshrrev_b32 v160, 1, v242\n\t" \
  "v_xor_b32 v160, v160, v241\n\t" \
  "v_lshlrev_b32 v160, 4, v160\n\t" \
  "v_lshl_add_u32 v160, v241, 8, v160\n\t" \
  "v_and_b32 v161, 1, v242\n\t" \
  "v_lshl_add_u32 v160, v161, 3, v160\n\t" \
  "s_lshl_b32 %[t0], %[w], 13\n\t" \
  "s_add_u32 %[t0], %[t0], 131072\n\t" \
  "v_add_u32 v160, %[t0], v160\n\t" \
  "v_xor_b32 v161, 32, v160\n\t" \
  "v_xor_b32 v162, 64, v160\n\t" \
  "v_xor_b32 v163, 96, v160\n\t" \
  "v_xor_b32 v164, 128, v160\n\t" \
  "v_xor_b32 v165, 160, v160\n\t" \
  "v_xor_b32 v166, 192, v160\n\t" \
  "v_xor_b32 v167, 224, v160\n\t" \
  "s_lshl_b32 %[t0], %[w], 6\n\t" \
  "v_xor_b32 v246, %[t0], v237\n\t" \
  "v_add_u32 v246, 40960, v246\n\t" \
  "v_xor_b32 v247, 32, v246\n\t" \
  "v_lshl_add_u32 v252, v242, 3, v243\n\t" \
  "v_lshlrev_b32 v252, 7, v252\n\t" \
  "v_lshl_add_u32 v252, v239, 3, v252\n\t" \
  "v_lshrrev_b32 v236, 1, v243\n\t" \
  "v_and_b32 v233, 1, v242\n\t" \
  "v_lshl_add_u32 v236, v233, 1, v236\n\t" \
  "v_xor_b32 v236, %[w], v236\n\t" \
  "v_lshl_add_u32 v252, v236, 5, v252\n\t" \
  "v_add_u32 v252, 131072, v252\n\t" \
  "v_add_u32 v253, 131072, v168\n\t" \
  "v_lshlrev_b32 v170, 2, v168\n\t" \
  "v_mov_b32 v0, 0\n\t" \
  "v_mov_b32 v1, 0\n\t" \
  "v_mov_b32 v2, 0\n\t" \
  "v_mov_b32 v3, 0\n\t" \
  "v_mov_b32 v4, 0\n\t" \
  "v_mov_b32 v5, 0\n\t" \
  "v_mov_b32 v6, 0\n\t" \
  "v_mov_b32 v7, 0\n\t" \
  "v_mov_b32 v8, 0\n\t" \
  "v_mov_b32 v9, 0\n\t" \
  "v_mov_b32 v10, 0\n\t" \
  "v_mov_b32 v11, 0\n\t" \
  "v_mov_b32 v12, 0\n\t" \
  "v_mov_b32 v13, 0\n\t" \
  "v_mov_b32 v14, 0\n\t" \
  "v_mov_b32 v15, 0\n\t" \
  "v_mov_b32 v16, 0\n\t" \
  "v_mov_b32 v17, 0\n\t" \
  "v_mov_b32 v18, 0\n\t" \
  "v_mov_b32 v19, 0\n\t" \
  "v_mov_b32 v20, 0\n\t" \
  "v_mov_b32 v21, 0\n\t" \
  "v_mov_b32 v22, 0\n\t" \
  "v_mov_b32 v23, 0\n\t" \
  "v_mov_b32 v24, 0\n\t" \
  "v_mov_b32 v25, 0\n\t" \
  "v_mov_b32 v26, 0\n\t" \
  "v_mov_b32 v27, 0\n\t" \
  "v_mov_b32 v28, 0\n\t" \
  "v_mov_b32 v29, 0\n\t" \
  "v_mov_b32 v30, 0\n\t" \
  "v_mov_b32 v31, 0\n\t" \
  "v_mov_b32 v32, 0\n\t" \
  "v_mov_b32 v33, 0\n\t" \
  "v_mov_b32 v34, 0\n\t" \
  "v_mov_b32 v35, 0\n\t" \
  "v_mov_b32 v36, 0\n\t" \
  "v_mov_b32 v37, 0\n\t" \
  "v_mov_b32 v38, 0\n\t" \
  "v_mov_b32 v39, 0\n\t" \
  "v_mov_b32 v40, 0\n\t" \
  "v_mov_b32 v41, 0\n\t" \
  "v_mov_b32 v42, 0\n\t" \
  "v_mov_b32 v43, 0\n\t" \
  "v_mov_b32 v44, 0\n\t" \
  "v_mov_b32 v45, 0\n\t" \
  "v_mov_b32 v46, 0\n\t" \
  "v_mov_b32 v47, 0\n\t" \
  "v_mov_b32 v48, 0\n\t" \
  "v_mov_b32 v49, 0\n\t" \
  "v_mov_b32 v50, 0\n\t" \
  "v_mov_b32 v51, 0\n\t" \
  "v_mov_b32 v52, 0\n\t" \
  "v_mov_b32 v53, 0\n\t" \
  "v_mov_b32 v54, 0\n\t" \
  "v_mov_b32 v55, 0\n\t" \
  "v_mov_b32 v56, 0\n\t" \
  "v_mov_b32 v57, 0\n\t" \
  "v_mov_b32 v58, 0\n\t" \
  "v_mov_b32 v59, 0\n\t" \
  "v_mov_b32 v60, 0\n\t" \
  "v_mov_b32 v61, 0\n\t" \
  "v_mov_b32 v62, 0\n\t" \
  "v_mov_b32 v63, 0\n\t" \
  "s_mov_b32 %[t1], 4\n\t" \
  "s_barrier\n\t" \
  "ds_read_b128 v[128:131], v244 offset:0\n\t" \
  "ds_read_b128 v[132:135], v244 offset:2048\n\t" \
  "ds_read_b64_tr_b16 v[64:65], v224 offset:0\n\t" \
  "ds_read_b64_tr_b16 v[66:67], v224 offset:1024\n\t" \
  "ds_read_b64_tr_b16 v[68:69], v225 offset:0\n\t" \
  "ds_read_b64_tr_b16 v[70:71], v225 offset:1024\n\t" \
  "ds_read_b64_tr_b16 v[72:73], v226 offset:0\n\t" \
  "ds_read_b64_tr_b16 v[74:75], v226 offset:1024\n\t" \
  "ds_read_b64_tr_b16 v[76:77], v227 offset:0\n\t" \
  "ds_read_b64_tr_b16 v[78:79], v227 offset:1024\n\t" \
  "ds_read_b64_tr_b16 v[80:81], v228 offset:0\n\t" \
  "ds_read_b64_tr_b16 v[82:83], v228 offset:1024\n\t" \
  "ds_read_b64_tr_b16 v[84:85], v229 offset:0\n\t" \
  "ds_read_b64_tr_b16 v[86:87], v229 offset:1024\n\t" \
  "ds_read_b64_tr_b16 v[88:89], v230 offset:0\n\t" \
  "ds_read_b64_tr_b16 v[90:91], v230 offset:1024\n\t" \
  "ds_read_b64_tr_b16 v[92:93], v231 offset:0\n\t" \
  "ds_read_b64_tr_b16 v[94:95], v231 offset:1024\n\t" \
  "s_branch .Lp1mid2_%=\n\t" \
  ".Lp1top1_%=:\n\t" \
  "s_waitcnt lgkmcnt(8)\n\t" \
  "s_barrier\n\t" \
  "v_mfma_f32_16x16x32_bf16 v[0:3], v[96:99], v[136:139], v[0:3]\n\t" \
  "ds_read_b128 v[128:131], v244 offset:0\n\t" \
  "ds_read_b128 v[132:135], v244 offset:2048\n\t" \
  "v_mfma_f32_16x16x32_bf16 v[4:7], v[96:99], v[140:143], v[4:7]\n\t" \
  "ds_read_b64_tr_b16 v[64:65], v224 offset:0\n\t" \
  "v_mfma_f32_16x16x32_bf16 v[8:11], v[100:103], v[136:139], v[8:11]\n\t" \
  "ds_read_b64_tr_b16 v[66:67], v224 offset:1024\n\t" \
  "v_mfma_f32_16x16x32_bf16 v[12:15], v[100:103], v[140:143], v[12:15]\n\t" \
  "ds_read_b64_tr_b16 v[68:69], v225 offset:0\n\t" \
  "v_mfma_f32_16x16x32_bf16 v[16:19], v[104:107], v[136:139], v[16:19]\n\t" \
  "ds_read_b64_tr_b16 v[70:71], v225 offset:1024\n\t" \
  "ds_read_b64_tr_b16 v[72:73], v226 offset:0\n\t" \
  "v_mfma_f32_16x16x32_bf16 v[20:23], v[104:107], v[140:143], v[20:23]\n\t" \
  "ds_read_b64_tr_b16 v[74:75], v226 offset:1024\n\t" \
  "v_mfma_f32_16x16x32_bf16 v[24:27], v[108:111], v[136:139], v[24:27]\n\t" \
  "ds_read_b64_tr_b16 v[76:77], v227 offset:0\n\t" \
  "v_mfma_f32_16x16x32_bf16 v[28:31], v[108:111], v[140:143], v[28:31]\n\t" \
  "ds_read_b64_tr_b16 v[78:79], v227 offset:1024\n\t" \
  "s_waitcnt lgkmcnt(10)\n\t" \
  "v_mfma_f32_16x16x32_bf16 v[32:35], v[112:115], v[136:139], v[32:35]\n\t" \
  "ds_read_b64_tr_b16 v[80:81], v228 offset:0\n\t" \
  "v_mfma_f32_16x16x32_bf16 v[36:39], v[112:115], v[140:143], v[36:39]\n\t" \
  "ds_read_b64_tr_b16 v[82:83], v228 offset:1024\n\t" \
  "v_mfma_f32_16x16x32_bf16 v[40:43], v[116:119], v[136:139], v[40:43]\n\t" \
  "ds_read_b64_tr_b16 v[84:85], v229 offset:0\n\t" \
  "v_mfma_f32_16x16x32_bf16 v[44:47], v[116:119], v[140:143], v[44:47]\n\t" \
  "ds_read_b64_tr_b16 v[86:87], v229 offset:1024\n\t" \
  "v_mfma_f32_16x16x32_bf16 v[48:51], v[120:123], v[136:139], v[48:51]\n\t" \
  "ds_read_b64_tr_b16 v[88:89], v230 offset:0\n\t" \
  "v_mfma_f32_16x16x32_bf16 v[52:55], v[120:123], v[140:143], v[52:55]\n\t" \
  "ds_read_b64_tr_b16 v[90:91], v230 offset:1024\n\t" \
  "v_mfma_f32_16x16x32_bf16 v[56:59], v[124:127], v[136:139], v[56:59]\n\t" \
  "ds_read_b64_tr_b16 v[92:93], v231 offset:0\n\t" \
  "v_mfma_f32_16x16x32_bf16 v[60:63], v[124:127], v[140:143], v[60:63]\n\t" \
  "ds_read_b64_tr_b16 v[94:95], v231 offset:1024\n\t" \
  ".Lp1mid2_%=:\n\t" \
  "s_waitcnt lgkmcnt(8)\n\t" \
  "v_mfma_f32_16x16x32_bf16 v[0:3], v[64:67], v[128:131], v[0:3]\n\t" \
  "ds_read_b128 v[136:139], v245 offset:0\n\t" \
  "ds_read_b128 v[140:143], v245 offset:2048\n\t" \
  "v_mfma_f32_16x16x32_bf16 v[4:7], v[64:67], v[132:135], v[4:7]\n\t" \
  "ds_read_b64_tr_b16 v[96:97], v224 offset:8192\n\t" \
  "v_mfma_f32_16x16x32_bf16 v[8:11], v[68:71], v[128:131], v[8:11]\n\t" \
  "ds_read_b64_tr_b16 v[98:99], v224 offset:9216\n\t" \
  "v_mfma_f32_16x16x32_bf16 v[12:15], v[68:71], v[132:135], v[12:15]\n\t" \
  "ds_read_b64_tr_b16 v[100:101], v225 offset:8192\n\t" \
  "v_mfma_f32_16x16x32_bf16 v[16:19], v[72:75], v[128:131], v[16:19]\n\t" \
  "ds_read_b64_tr_b16 v[102:103], v225 offset:9216\n\t" \
  "ds_read_b64_tr_b16 v[104:105], v226 offset:8192\n\t" \
  "v_mfma_f32_16x16x32_bf16 v[20:23], v[72:75], v[132:135], v[20:23]\n\t" \
  "ds_read_b64_tr_b16 v[106:107], v226 offset:9216\n\t" \
  "v_mfma_f32_16x16x32_bf16 v[24:27], v[76:79], v[128:131], v[24:27]\n\t" \
  "ds_read_b64_tr_b16 v[108:109], v227 offset:8192\n\t" \
  "v_mfma_f32_16x16x32_bf16 v[28:31], v[76:79], v[132:135], v[28:31]\n\t" \
  "ds_read_b64_tr_b16 v[110:111], v227 offset:9216\n\t" \
  "s_waitcnt lgkmcnt(10)\n\t" \
  "v_mfma_f32_16x16x32_bf16 v[32:35], v[80:83], v[128:131], v[32:35]\n\t" \
  "ds_read_b64_tr_b16 v[112:113], v228 offset:8192\n\t" \
  "v_mfma_f32_16x16x32_bf16 v[36:39], v[80:83], v[132:135], v[36:39]\n\t" \
  "ds_read_b64_tr_b16 v[114:115], v228 offset:9216\n\t" \
  "v_mfma_f32_16x16x32_bf16 v[40:43], v[84:87], v[128:131], v[40:43]\n\t" \
  "ds_read_b64_tr_b16 v[116:117], v229 offset:8192\n\t" \
  "v_mfma_f32_16x16x32_bf16 v[44:47], v[84:87], v[132:135], v[44:47]\n\t" \
  "ds_read_b64_tr_b16 v[118:119], v229 offset:9216\n\t" \
  "v_mfma_f32_16x16x32_bf16 v[48:51], v[88:91], v[128:131], v[48:51]\n\t" \
  "ds_read_b64_tr_b16 v[120:121], v230 offset:8192\n\t" \
  "v_mfma_f32_16x16x32_bf16 v[52:55], v[88:91], v[132:135], v[52:55]\n\t" \
  "ds_read_b64_tr_b16 v[122:123], v230 offset:9216\n\t" \
  "v_mfma_f32_16x16x32_bf16 v[56:59], v[92:95], v[128:131], v[56:59]\n\t" \
  "ds_read_b64_tr_b16 v[124:125], v231 offset:8192\n\t" \
  "v_mfma_f32_16x16x32_bf16 v[60:63], v[92:95], v[132:135], v[60:63]\n\t" \
  "ds_read_b64_tr_b16 v[126:127], v231 offset:9216\n\t" \
  "s_waitcnt lgkmcnt(8)\n\t" \
  "s_barrier\n\t" \
  "v_mfma_f32_16x16x32_bf16 v[0:3], v[96:99], v[136:139], v[0:3]\n\t" \
  "ds_read_b128 v[128:131], v244 offset:32768\n\t" \
  "ds_read_b128 v[132:135], v244 offset:34816\n\t" \
  "v_mfma_f32_16x16x32_bf16 v[4:7], v[96:99], v[140:143], v[4:7]\n\t" \
  "ds_read_b64_tr_b16 v[64:65], v224 offset:32768\n\t" \
  "v_mfma_f32_16x16x32_bf16 v[8:11], v[100:103], v[136:139], v[8:11]\n\t" \
  "ds_read_b64_tr_b16 v[66:67], v224 offset:33792\n\t" \
  "v_mfma_f32_16x16x32_bf16 v[12:15], v[100:103], v[140:143], v[12:15]\n\t" \
  "ds_read_b64_tr_b16 v[68:69], v225 offset:32768\n\t" \
  "v_mfma_f32_16x16x32_bf16 v[16:19], v[104:107], v[136:139], v[16:19]\n\t" \
  "ds_read_b64_tr_b16 v[70:71], v225 offset:33792\n\t" \
  "ds_read_b64_tr_b16 v[72:73], v226 offset:32768\n\t" \
  "v_mfma_f32_16x16x32_bf16 v[20:23], v[104:107], v[140:143], v[20:23]\n\t" \
  "ds_read_b64_tr_b16 v[74:75], v226 offset:33792\n\t" \
  "v_mfma_f32_16x16x32_bf16 v[24:27], v[108:111], v[136:139], v[24:27]\n\t" \
  "ds_read_b64_tr_b16 v[76:77], v227 offset:32768\n\t" \
  "v_mfma_f32_16x16x32_bf16 v[28:31], v[108:111], v[140:143], v[28:31]\n\t" \
  "ds_read_b64_tr_b16 v[78:79], v227 offset:33792\n\t" \
  "s_waitcnt lgkmcnt(10)\n\t" \
  "v_mfma_f32_16x16x32_bf16 v[32:35], v[112:115], v[136:139], v[32:35]\n\t" \
  "ds_read_b64_tr_b16 v[80:81], v228 offset:32768\n\t" \
  "v_mfma_f32_16x16x32_bf16 v[36:39], v[112:115], v[140:143], v[36:39]\n\t" \
  "ds_read_b64_tr_b16 v[82:83], v228 offset:33792\n\t" \
  "v_mfma_f32_16x16x32_bf16 v[40:43], v[116:119], v[136:139], v[40:43]\n\t" \
  "ds_read_b64_tr_b16 v[84:85], v229 offset:32768\n\t" \
  "v_mfma_f32_16x16x32_bf16 v[44:47], v[116:119], v[140:143], v[44:47]\n\t" \
  "ds_read_b64_tr_b16 v[86:87], v229 offset:33792\n\t" \
  "v_xor_b32 v244, 0x10000, v244\n\t" \
  "v_mfma_f32_16x16x32_bf16 v[48:51], v[120:123], v[136:139], v[48:51]\n\t" \
  "ds_read_b64_tr_b16 v[88:89], v230 offset:32768\n\t" \
  "v_mfma_f32_16x16x32_bf16 v[52:55], v[120:123], v[140:143], v[52:55]\n\t" \
  "ds_read_b64_tr_b16 v[90:91], v230 offset:33792\n\t" \
  "v_mfma_f32_16x16x32_bf16 v[56:59], v[124:127], v[136:139], v[56:59]\n\t" \
  "ds_read_b64_tr_b16 v[92:93], v231 offset:32768\n\t" \
  "v_mfma_f32_16x16x32_bf16 v[60:63], v[124:127], v[140:143], v[60:63]\n\t" \
  "ds_read_b64_tr_b16 v[94:95], v231 offset:33792\n\t" \
  "s_waitcnt lgkmcnt(8)\n\t" \
  "v_mfma_f32_16x16x32_bf16 v[0:3], v[64:67], v[128:131], v[0:3]\n\t" \
  "ds_read_b128 v[136:139], v245 offset:32768\n\t" \
  "ds_read_b128 v[140:143], v245 offset:34816\n\t" \
  "v_mfma_f32_16x16x32_bf16 v[4:7], v[64:67], v[132:135], v[4:7]\n\t" \
  "ds_read_b64_tr_b16 v[96:97], v224 offset:40960\n\t" \
  "v_mfma_f32_16x16x32_bf16 v[8:11], v[68:71], v[128:131], v[8:11]\n\t" \
  "ds_read_b64_tr_b16 v[98:99], v224 offset:41984\n\t" \
  "v_mfma_f32_16x16x32_bf16 v[12:15], v[68:71], v[132:135], v[12:15]\n\t" \
  "ds_read_b64_tr_b16 v[100:101], v225 offset:40960\n\t" \
  "v_mfma_f32_16x16x32_bf16 v[16:19], v[72:75], v[128:131], v[16:19]\n\t" \
  "ds_read_b64_tr_b16 v[102:103], v225 offset:41984\n\t" \
  "ds_read_b64_tr_b16 v[104:105], v226 offset:40960\n\t" \
  "v_mfma_f32_16x16x32_bf16 v[20:23], v[72:75], v[132:135], v[20:23]\n\t" \
  "ds_read_b64_tr_b16 v[106:107], v226 offset:41984\n\t" \
  "v_mfma_f32_16x16x32_bf16 v[24:27], v[76:79], v[128:131], v[24:27]\n\t" \
  "ds_read_b64_tr_b16 v[108:109], v227 offset:40960\n\t" \
  "v_mfma_f32_16x16x32_bf16 v[28:31], v[76:79], v[132:135], v[28:31]\n\t" \
  "ds_read_b64_tr_b16 v[110:111], v227 offset:41984\n\t" \
  "s_waitcnt lgkmcnt(10)\n\t" \
  "v_mfma_f32_16x16x32_bf16 v[32:35], v[80:83], v[128:131], v[32:35]\n\t" \
  "ds_read_b64_tr_b16 v[112:113], v228 offset:40960\n\t" \
  "v_xor_b32 v245, 0x10000, v245\n\t" \
  "v_mfma_f32_16x16x32_bf16 v[36:39], v[80:83], v[132:135], v[36:39]\n\t" \
  "ds_read_b64_tr_b16 v[114:115], v228 offset:41984\n\t" \
  "v_xor_b32 v224, 0x10000, v224\n\t" \
  "v_mfma_f32_16x16x32_bf16 v[40:43], v[84:87], v[128:131], v[40:43]\n\t" \
  "ds_read_b64_tr_b16 v[116:117], v229 offset:40960\n\t" \
  "v_xor_b32 v225, 0x10000, v225\n\t" \
  "v_xor_b32 v228, 0x10000, v228\n\t" \
  "v_mfma_f32_16x16x32_bf16 v[44:47], v[84:87], v[132:135], v[44:47]\n\t" \
  "ds_read_b64_tr_b16 v[118:119], v229 offset:41984\n\t" \
  "v_xor_b32 v226, 0x10000, v226\n\t" \
  "v_mfma_f32_16x16x32_bf16 v[48:51], v[88:91], v[128:131], v[48:51]\n\t" \
  "ds_read_b64_tr_b16 v[120:121], v230 offset:40960\n\t" \
  "v_xor_b32 v227, 0x10000, v227\n\t" \
  "v_xor_b32 v229, 0x10000, v229\n\t" \
  "v_mfma_f32_16x16x32_bf16 v[52:55], v[88:91], v[132:135], v[52:55]\n\t" \
  "ds_read_b64_tr_b16 v[122:123], v230 offset:41984\n\t" \
  "v_mfma_f32_16x16x32_bf16 v[56:59], v[92:95], v[128:131], v[56:59]\n\t" \
  "ds_read_b64_tr_b16 v[124:125], v231 offset:40960\n\t" \
  "v_xor_b32 v230, 0x10000, v230\n\t" \
  "v_mfma_f32_16x16x32_bf16 v[60:63], v[92:95], v[132:135], v[60:63]\n\t" \
  "ds_read_b64_tr_b16 v[126:127], v231 offset:41984\n\t" \
  "v_xor_b32 v231, 0x10000, v231\n\t" \
  "s_sub_u32 %[t1], %[t1], 1\n\t" \
  "s_cmp_lg_u32 %[t1], 0\n\t" \
  "s_cbranch_scc1 .Lp1top1_%=\n\t" \
  "s_waitcnt lgkmcnt(8)\n\t" \
  "s_barrier\n\t" \
  "v_mfma_f32_16x16x32_bf16 v[0:3], v[96:99], v[136:139], v[0:3]\n\t" \
  "ds_read_b128 v[128:131], v244 offset:0\n\t" \
  "ds_read_b128 v[132:135], v244 offset:2048\n\t" \
  "v_mfma_f32_16x16x32_bf16 v[4:7], v[96:99], v[140:143], v[4:7]\n\t" \
  "ds_read_b64_tr_b16 v[64:65], v224 offset:0\n\t" \
  "v_mfma_f32_16x16x32_bf16 v[8:11], v[100:103], v[136:139], v[8:11]\n\t" \
  "ds_read_b64_tr_b16 v[66:67], v224 offset:1024\n\t" \
  "v_mfma_f32_16x16x32_bf16 v[12:15], v[100:103], v[140:143], v[12:15]\n\t" \
  "ds_read_b64_tr_b16 v[68:69], v225 offset:0\n\t" \
  "v_mfma_f32_16x16x32_bf16 v[16:19], v[104:107], v[136:139], v[16:19]\n\t" \
  "ds_read_b64_tr_b16 v[70:71], v225 offset:1024\n\t" \
  "ds_read_b64_tr_b16 v[72:73], v226 offset:0\n\t" \
  "v_mfma_f32_16x16x32_bf16 v[20:23], v[104:107], v[140:143], v[20:23]\n\t" \
  "ds_read_b64_tr_b16 v[74:75], v226 offset:1024\n\t" \
  "v_mfma_f32_16x16x32_bf16 v[24:27], v[108:111], v[136:139], v[24:27]\n\t" \
  "ds_read_b64_tr_b16 v[76:77], v227 offset:0\n\t" \
  "v_mfma_f32_16x16x32_bf16 v[28:31], v[108:111], v[140:143], v[28:31]\n\t" \
  "ds_read_b64_tr_b16 v[78:79], v227 offset:1024\n\t" \
  "s_waitcnt lgkmcnt(10)\n\t" \
  "v_mfma_f32_16x16x32_bf16 v[32:35], v[112:115], v[136:139], v[32:35]\n\t" \
  "ds_read_b64_tr_b16 v[80:81], v228 offset:0\n\t" \
  "v_mfma_f32_16x16x32_bf16 v[36:39], v[112:115], v[140:143], v[36:39]\n\t" \
  "ds_read_b64_tr_b16 v[82:83], v228 offset:1024\n\t" \
  "v_mfma_f32_16x16x32_bf16 v[40:43], v[116:119], v[136:139], v[40:43]\n\t" \
  "ds_read_b64_tr_b16 v[84:85], v229 offset:0\n\t" \
  "v_mfma_f32_16x16x32_bf16 v[44:47], v[116:119], v[140:143], v[44:47]\n\t" \
  "ds_read_b64_tr_b16 v[86:87], v229 offset:1024\n\t" \
  "v_mfma_f32_16x16x32_bf16 v[48:51], v[120:123], v[136:139], v[48:51]\n\t" \
  "ds_read_b64_tr_b16 v[88:89], v230 offset:0\n\t" \
  "v_mfma_f32_16x16x32_bf16 v[52:55], v[120:123], v[140:143], v[52:55]\n\t" \
  "ds_read_b64_tr_b16 v[90:91], v230 offset:1024\n\t" \
  "v_mfma_f32_16x16x32_bf16 v[56:59], v[124:127], v[136:139], v[56:59]\n\t" \
  "ds_read_b64_tr_b16 v[92:93], v231 offset:0\n\t" \
  "v_mfma_f32_16x16x32_bf16 v[60:63], v[124:127], v[140:143], v[60:63]\n\t" \
  "ds_read_b64_tr_b16 v[94:95], v231 offset:1024\n\t" \
  "s_waitcnt lgkmcnt(8)\n\t" \
  "v_mfma_f32_16x16x32_bf16 v[0:3], v[64:67], v[128:131], v[0:3]\n\t" \
  "ds_read_b128 v[136:139], v245 offset:0\n\t" \
  "ds_read_b128 v[140:143], v245 offset:2048\n\t" \
  "v_mfma_f32_16x16x32_bf16 v[4:7], v[64:67], v[132:135], v[4:7]\n\t" \
  "ds_read_b64_tr_b16 v[96:97], v224 offset:8192\n\t" \
  "v_mfma_f32_16x16x32_bf16 v[8:11], v[68:71], v[128:131], v[8:11]\n\t" \
  "ds_read_b64_tr_b16 v[98:99], v224 offset:9216\n\t" \
  "v_mfma_f32_16x16x32_bf16 v[12:15], v[68:71], v[132:135], v[12:15]\n\t" \
  "ds_read_b64_tr_b16 v[100:101], v225 offset:8192\n\t" \
  "v_mfma_f32_16x16x32_bf16 v[16:19], v[72:75], v[128:131], v[16:19]\n\t" \
  "ds_read_b64_tr_b16 v[102:103], v225 offset:9216\n\t" \
  "ds_read_b64_tr_b16 v[104:105], v226 offset:8192\n\t" \
  "v_mfma_f32_16x16x32_bf16 v[20:23], v[72:75], v[132:135], v[20:23]\n\t" \
  "ds_read_b64_tr_b16 v[106:107], v226 offset:9216\n\t" \
  "v_mfma_f32_16x16x32_bf16 v[24:27], v[76:79], v[128:131], v[24:27]\n\t" \
  "ds_read_b64_tr_b16 v[108:109], v227 offset:8192\n\t" \
  "v_mfma_f32_16x16x32_bf16 v[28:31], v[76:79], v[132:135], v[28:31]\n\t" \
  "ds_read_b64_tr_b16 v[110:111], v227 offset:9216\n\t" \
  "s_waitcnt lgkmcnt(10)\n\t" \
  "v_mfma_f32_16x16x32_bf16 v[32:35], v[80:83], v[128:131], v[32:35]\n\t" \
  "ds_read_b64_tr_b16 v[112:113], v228 offset:8192\n\t" \
  "v_mfma_f32_16x16x32_bf16 v[36:39], v[80:83], v[132:135], v[36:39]\n\t" \
  "ds_read_b64_tr_b16 v[114:115], v228 offset:9216\n\t" \
  "v_mfma_f32_16x16x32_bf16 v[40:43], v[84:87], v[128:131], v[40:43]\n\t" \
  "ds_read_b64_tr_b16 v[116:117], v229 offset:8192\n\t" \
  "v_mfma_f32_16x16x32_bf16 v[44:47], v[84:87], v[132:135], v[44:47]\n\t" \
  "ds_read_b64_tr_b16 v[118:119], v229 offset:9216\n\t" \
  "v_mfma_f32_16x16x32_bf16 v[48:51], v[88:91], v[128:131], v[48:51]\n\t" \
  "ds_read_b64_tr_b16 v[120:121], v230 offset:8192\n\t" \
  "v_mfma_f32_16x16x32_bf16 v[52:55], v[88:91], v[132:135], v[52:55]\n\t" \
  "ds_read_b64_tr_b16 v[122:123], v230 offset:9216\n\t" \
  "v_mfma_f32_16x16x32_bf16 v[56:59], v[92:95], v[128:131], v[56:59]\n\t" \
  "ds_read_b64_tr_b16 v[124:125], v231 offset:8192\n\t" \
  "v_mfma_f32_16x16x32_bf16 v[60:63], v[92:95], v[132:135], v[60:63]\n\t" \
  "ds_read_b64_tr_b16 v[126:127], v231 offset:9216\n\t" \
  "s_waitcnt lgkmcnt(8)\n\t" \
  "s_barrier\n\t" \
  "v_mfma_f32_16x16x32_bf16 v[0:3], v[96:99], v[136:139], v[0:3]\n\t" \
  "ds_read_b64 v[64:65], v160 offset:0\n\t" \
  "s_waitcnt vmcnt(0)\n\t" \
  "v_mfma_f32_16x16x32_bf16 v[4:7], v[96:99], v[140:143], v[4:7]\n\t" \
  "ds_read_b64 v[66:67], v161 offset:0\n\t" \
  "v_readfirstlane_b32 %[t0], v176\n\t" \
  "v_mfma_f32_16x16x32_bf16 v[8:11], v[100:103], v[136:139], v[8:11]\n\t" \
  "ds_read_b64 v[68:69], v162 offset:0\n\t" \
  "v_readfirstlane_b32 %[t1], v177\n\t" \
  "v_mfma_f32_16x16x32_bf16 v[12:15], v[100:103], v[140:143], v[12:15]\n\t" \
  "ds_read_b64 v[70:71], v163 offset:0\n\t" \
  "s_mul_hi_u32 %[thi], %[t0], 0x7f4a7c15\n\t" \
  "s_mul_i32 %[t2], %[t0], 0x9e3779b9\n\t" \
  "v_mfma_f32_16x16x32_bf16 v[16:19], v[104:107], v[136:139], v[16:19]\n\t" \
  "ds_read_b64 v[72:73], v164 offset:0\n\t" \
  "s_add_u32 %[thi], %[thi], %[t2]\n\t" \
  "s_mul_i32 %[t2], %[t1], 0x7f4a7c15\n\t" \
  "v_mfma_f32_16x16x32_bf16 v[20:23], v[104:107], v[140:143], v[20:23]\n\t" \
  "ds_read_b64 v[74:75], v165 offset:0\n\t" \
  "s_add_u32 %[thi], %[thi], %[t2]\n\t" \
  "s_mul_i32 %[tlo], %[t0], 0x7f4a7c15\n\t" \
  "v_mfma_f32_16x16x32_bf16 v[24:27], v[108:111], v[136:139], v[24:27]\n\t" \
  "ds_read_b64 v[76:77], v166 offset:0\n\t" \
  "s_add_u32 %[tlo], %[tlo], 0xd192ed03\n\t" \
  "s_addc_u32 %[thi], %[thi], 0xd1b54a32\n\t" \
  "v_mfma_f32_16x16x32_bf16 v[28:31], v[108:111], v[140:143], v[28:31]\n\t" \
  "ds_read_b64 v[78:79], v167 offset:0\n\t" \
  "s_waitcnt lgkmcnt(8)\n\t" \
  "v_mfma_f32_16x16x32_bf16 v[32:35], v[112:115], v[136:139], v[32:35]\n\t" \
  "ds_read_b64 v[80:81], v160 offset:4096\n\t" \
  "v_cvt_pk_bf16_f32 v0, v0, v1\n\t" \
  "v_cvt_pk_bf16_f32 v1, v2, v3\n\t" \
  "v_mfma_f32_16x16x32_bf16 v[36:39], v[112:115], v[140:143], v[36:39]\n\t" \
  "ds_read_b64 v[82:83], v161 offset:4096\n\t" \
  "v_cvt_pk_bf16_f32 v2, v8, v9\n\t" \
  "v_cvt_pk_bf16_f32 v3, v10, v11\n\t" \
  "v_mfma_f32_16x16x32_bf16 v[40:43], v[116:119], v[136:139], v[40:43]\n\t" \
  "ds_read_b64 v[84:85], v162 offset:4096\n\t" \
  "v_cvt_pk_bf16_f32 v4, v4, v5\n\t" \
  "v_cvt_pk_bf16_f32 v5, v6, v7\n\t" \
  "v_mfma_f32_16x16x32_bf16 v[44:47], v[116:119], v[140:143], v[44:47]\n\t" \
  "ds_read_b64 v[86:87], v163 offset:4096\n\t" \
  "v_cvt_pk_bf16_f32 v6, v12, v13\n\t" \
  "v_cvt_pk_bf16_f32 v7, v14, v15\n\t" \
  "v_mfma_f32_16x16x32_bf16 v[48:51], v[120:123], v[136:139], v[48:51]\n\t" \
  "ds_read_b64 v[88:89], v164 offset:4096\n\t" \
  "v_cvt_pk_bf16_f32 v16, v16, v17\n\t" \
  "v_cvt_pk_bf16_f32 v17, v18, v19\n\t" \
  "v_mfma_f32_16x16x32_bf16 v[52:55], v[120:123], v[140:143], v[52:55]\n\t" \
  "ds_read_b64 v[90:91], v165 offset:4096\n\t" \
  "v_cvt_pk_bf16_f32 v18, v24, v25\n\t" \
  "v_cvt_pk_bf16_f32 v19, v26, v27\n\t" \
  "v_mfma_f32_16x16x32_bf16 v[56:59], v[124:127], v[136:139], v[56:59]\n\t" \
  "ds_read_b64 v[92:93], v166 offset:4096\n\t" \
  "v_cvt_pk_bf16_f32 v20, v20, v21\n\t" \
  "v_cvt_pk_bf16_f32 v21, v22, v23\n\t" \
  "v_mfma_f32_16x16x32_bf16 v[60:63], v[124:127], v[140:143], v[60:63]\n\t" \
  "ds_read_b64 v[94:95], v167 offset:4096\n\t" \
  "v_cvt_pk_bf16_f32 v22, v28, v29\n\t" \
  "v_cvt_pk_bf16_f32 v23, v30, v31\n\t" \
  "s_waitcnt lgkmcnt(4)\n\t" \
  "v_mfma_f32_16x16x32_bf16 v[144:147], v[64:67], v[0:3], 0\n\t" \
  "v_cvt_pk_bf16_f32 v32, v32, v33\n\t" \
  "v_cvt_pk_bf16_f32 v33, v34, v35\n\t" \
  "v_mfma_f32_16x16x32_bf16 v[148:151], v[80:83], v[0:3], 0\n\t" \
  "v_cvt_pk_bf16_f32 v34, v40, v41\n\t" \
  "v_cvt_pk_bf16_f32 v35, v42, v43\n\t" \
  "v_mfma_f32_16x16x32_bf16 v[152:155], v[64:67], v[4:7], 0\n\t" \
  "v_cvt_pk_bf16_f32 v36, v36, v37\n\t" \
  "v_cvt_pk_bf16_f32 v37, v38, v39\n\t" \
  "v_mfma_f32_16x16x32_bf16 v[156:159], v[80:83], v[4:7], 0\n\t" \
  "v_cvt_pk_bf16_f32 v38, v44, v45\n\t" \
  "v_cvt_pk_bf16_f32 v39, v46, v47\n\t" \
  "v_mfma_f32_16x16x32_bf16 v[144:147], v[68:71], v[16:19], v[144:147]\n\t" \
  "v_cvt_pk_bf16_f32 v48, v48, v49\n\t" \
  "v_cvt_pk_bf16_f32 v49, v50, v51\n\t" \
  "v_mfma_f32_16x16x32_bf16 v[148:151], v[84:87], v[16:19], v[148:151]\n\t" \
  "v_cvt_pk_bf16_f32 v50, v56, v57\n\t" \
  "v_cvt_pk_bf16_f32 v51, v58, v59\n\t" \
  "v_mfma_f32_16x16x32_bf16 v[152:155], v[68:71], v[20:23], v[152:155]\n\t" \
  "v_cvt_pk_bf16_f32 v52, v52, v53\n\t" \
  "v_cvt_pk_bf16_f32 v53, v54, v55\n\t" \
  "v_mfma_f32_16x16x32_bf16 v[156:159], v[84:87], v[20:23], v[156:159]\n\t" \
  "v_cvt_pk_bf16_f32 v54, v60, v61\n\t" \
  "v_cvt_pk_bf16_f32 v55, v62, v63\n\t" \
  "s_waitcnt lgkmcnt(0)\n\t" \
  "s_barrier\n\t" \
  "v_mov_b32 v174, %[lown]\n\t" \
  "ds_write_b32 v174, v254\n\t" \
  "v_mfma_f32_16x16x32_bf16 v[144:147], v[72:75], v[32:35], v[144:147]\n\t" \
  "v_add_u32 v244, 32768, v238\n\t" \
  "v_mfma_f32_16x16x32_bf16 v[148:151], v[88:91], v[32:35], v[148:151]\n\t" \
  "v_mfma_f32_16x16x32_bf16 v[152:155], v[72:75], v[36:39], v[152:155]\n\t" \
  "v_xor_b32 v245, 64, v244\n\t" \
  "v_mfma_f32_16x16x32_bf16 v[156:159], v[88:91], v[36:39], v[156:159]\n\t" \
  "v_mfma_f32_16x16x32_bf16 v[144:147], v[76:79], v[48:51], v[144:147]\n\t" \
  "v_mfma_f32_16x16x32_bf16 v[148:151], v[92:95], v[48:51], v[148:151]\n\t" \
  "s_mov_b32 %[t0], 0x3e0293ee\n\t" \
  "v_mfma_f32_16x16x32_bf16 v[152:155], v[76:79], v[52:55], v[152:155]\n\t" \
  "v_mfma_f32_16x16x32_bf16 v[156:159], v[92:95], v[52:55], v[156:159]\n\t" \
  "s_nop 3\n\t" \
  "v_max3_f32 v160, v144, v145, v146\n\t" \
  "s_nop 1\n\t" \
  "v_max3_f32 v164, v152, v153, v154\n\t" \
  "v_max3_f32 v161, v147, v148, v149\n\t" \
  "v_max3_f32 v165, v155, v156, v157\n\t" \
  "v_max3_f32 v160, v160, v150, v151\n\t" \
  "v_max3_f32 v164, v164, v158, v159\n\t" \
  "v_max_f32 v160, v160, v161\n\t" \
  "v_max_f32 v164, v164, v165\n\t" \
  "v_mov_b32 v161, v160\n\t" \
  "v_mov_b32 v165, v164\n\t" \
  "s_nop 0\n\t" \
  "v_permlane16_swap_b32 v160, v161\n\t" \
  "v_permlane16_swap_b32 v164, v165\n\t" \
  "v_max_f32 v160, v160, v161\n\t" \
  "v_max_f32 v164, v164, v165\n\t" \
  "v_mov_b32 v161, v160\n\t" \
  "v_mov_b32 v165, v164\n\t" \
  "s_nop 0\n\t" \
  "v_permlane32_swap_b32 v160, v161\n\t" \
  "v_permlane32_swap_b32 v164, v165\n\t" \
  "v_max_f32 v160, v160, v161\n\t" \
  "v_max_f32 v164, v164, v165\n\t" \
  "v_mul_f32 v160, %[t0], v160\n\t" \
  "v_mul_f32 v164, %[t0], v164\n\t" \
  "v_fma_f32 v144, v144, %[t0], -v160\n\t" \
  "v_fma_f32 v152, v152, %[t0], -v164\n\t" \
  "v_fma_f32 v145, v145, %[t0], -v160\n\t" \
  "v_fma_f32 v153, v153, %[t0], -v164\n\t" \
  "v_fma_f32 v146, v146, %[t0], -v160\n\t" \
  "v_fma_f32 v154, v154, %[t0], -v164\n\t" \
  "v_fma_f32 v147, v147, %[t0], -v160\n\t" \
  "v_fma_f32 v155, v155, %[t0], -v164\n\t" \
  "v_fma_f32 v148, v148, %[t0], -v160\n\t" \
  "v_fma_f32 v156, v156, %[t0], -v164\n\t" \
  "v_fma_f32 v149, v149, %[t0], -v160\n\t" \
  "v_fma_f32 v157, v157, %[t0], -v164\n\t" \
  "v_fma_f32 v150, v150, %[t0], -v160\n\t" \
  "v_fma_f32 v158, v158, %[t0], -v164\n\t" \
  "v_fma_f32 v151, v151, %[t0], -v160\n\t" \
  "v_fma_f32 v159, v159, %[t0], -v164\n\t" \
  "v_exp_f32 v144, v144\n\t" \
  "v_exp_f32 v152, v152\n\t" \
  "v_exp_f32 v145, v145\n\t" \
  "v_exp_f32 v153, v153\n\t" \
  "v_exp_f32 v146, v146\n\t" \
  "v_exp_f32 v154, v154\n\t" \
  "v_exp_f32 v147, v147\n\t" \
  "v_exp_f32 v155, v155\n\t" \
  "v_exp_f32 v148, v148\n\t" \
  "v_exp_f32 v156, v156\n\t" \
  "v_exp_f32 v149, v149\n\t" \
  "v_exp_f32 v157, v157\n\t" \
  "v_exp_f32 v150, v150\n\t" \
  "v_exp_f32 v158, v158\n\t" \
  "v_exp_f32 v151, v151\n\t" \
  "v_exp_f32 v159, v159\n\t" \
  "v_add_f32 v160, v144, v145\n\t" \
  "v_add_f32 v164, v152, v153\n\t" \
  "v_add_f32 v162, v146, v147\n\t" \
  "v_add_f32 v166, v154, v155\n\t" \
  "v_add_f32 v163, v148, v149\n\t" \
  "v_add_f32 v167, v156, v157\n\t" \
  "v_add_f32 v160, v160, v162\n\t" \
  "v_add_f32 v164, v164, v166\n\t" \
  "v_add_f32 v162, v150, v151\n\t" \
  "v_add_f32 v166, v158, v159\n\t" \
  "v_add_f32 v163, v163, v162\n\t" \
  "v_add_f32 v167, v167, v166\n\t" \
  "v_add_f32 v160, v160, v163\n\t" \
  "v_add_f32 v164, v164, v167\n\t" \
  "v_mov_b32 v161, v160\n\t" \
  "v_mov_b32 v165, v164\n\t" \
  "s_nop 0\n\t" \
  "v_permlane16_swap_b32 v160, v161\n\t" \
  "v_permlane16_swap_b32 v164, v165\n\t" \
  "v_add_f32 v160, v160, v161\n\t" \
  "v_add_f32 v164, v164, v165\n\t" \
  "v_mov_b32 v161, v160\n\t" \
  "v_mov_b32 v165, v164\n\t" \
  "s_nop 0\n\t" \
  "v_permlane32_swap_b32 v160, v161\n\t" \
  "v_permlane32_swap_b32 v164, v165\n\t" \
  "v_add_f32 v160, v160, v161\n\t" \
  "v_add_f32 v164, v164, v165\n\t" \
  "v_rcp_f32 v160, v160\n\t" \
  "v_rcp_f32 v164, v164\n\t" \
  "v_mul_f32 v144, v144, v160\n\t" \
  "v_mul_f32 v152, v152, v164\n\t" \
  "v_mul_f32 v145, v145, v160\n\t" \
  "v_mul_f32 v153, v153, v164\n\t" \
  "v_mul_f32 v146, v146, v160\n\t" \
  "v_mul_f32 v154, v154, v164\n\t" \
  "v_mul_f32 v147, v147, v160\n\t" \
  "v_mul_f32 v155, v155, v164\n\t" \
  "v_mul_f32 v148, v148, v160\n\t" \
  "v_mul_f32 v156, v156, v164\n\t" \
  "v_mul_f32 v149, v149, v160\n\t" \
  "v_mul_f32 v157, v157, v164\n\t" \
  "v_mul_f32 v150, v150, v160\n\t" \
  "v_mul_f32 v158, v158, v164\n\t" \
  "v_mul_f32 v151, v151, v160\n\t" \
  "v_mul_f32 v159, v159, v164\n\t" \
  "v_cvt_pk_bf16_f32 v144, v144, v145\n\t" \
  "v_cvt_pk_bf16_f32 v145, v146, v147\n\t" \
  "v_cvt_pk_bf16_f32 v146, v148, v149\n\t" \
  "v_cvt_pk_bf16_f32 v147, v150, v151\n\t" \
  "v_cvt_pk_bf16_f32 v152, v152, v153\n\t" \
  "v_cvt_pk_bf16_f32 v153, v154, v155\n\t" \
  "v_cvt_pk_bf16_f32 v154, v156, v157\n\t" \
  "v_cvt_pk_bf16_f32 v155, v158, v159\n\t" \
  "buffer_store_dwordx4 v[144:147], v168, %[rw], %[spown] offen offset:0 sc1\n\t" \
  "buffer_store_dwordx4 v[152:155], v168, %[rw], %[spown] offen offset:1024 sc1\n\t" \
  "v_mov_b32 v82, v170\n\t" \
  "v_mov_b32 v84, %[tlo]\n\t" \
  "v_mov_b32 v85, %[thi]\n\t" \
  "s_barrier\n\t" \
  "ds_read_b64_tr_b16 v[176:177], v246 offset:0\n\t" \
  "ds_read_b64_tr_b16 v[178:179], v246 offset:1024\n\t" \
  "ds_read_b128 v[160:163], v244 offset:0\n\t" \
  "ds_read_b128 v[164:167], v244 offset:2048\n\t" \
  "ds_read_b128 v[168:171], v244 offset:4096\n\t" \
  "ds_read_b128 v[172:175], v244 offset:6144\n\t" \
  "ds_read_b64_tr_b16 v[180:181], v247 offset:0\n\t" \
  "ds_read_b64_tr_b16 v[182:183], v247 offset:1024\n\t" \
  "ds_read_b64_tr_b16 v[184:185], v252 offset:0\n\t" \
  "ds_read_b64_tr_b16 v[186:187], v252 offset:512\n\t" \
  "ds_read_b64_tr_b16 v[204:205], v246 offset:8192\n\t" \
  "ds_read_b64_tr_b16 v[206:207], v246 offset:9216\n\t" \
  "ds_read_b128 v[188:191], v245 offset:0\n\t" \
  "ds_read_b128 v[192:195], v245 offset:2048\n\t" \
  "ds_read_b128 v[196:199], v245 offset:4096\n\t" \
  "ds_read_b128 v[200:203], v245 offset:6144\n\t" \
  "ds_read_b64_tr_b16 v[208:209], v247 offset:8192\n\t" \
  "ds_read_b64_tr_b16 v[210:211], v247 offset:9216\n\t" \
  "ds_read_b64_tr_b16 v[212:213], v252 offset:4096\n\t" \
  "ds_read_b64_tr_b16 v[214:215], v252 offset:4608\n\t" \
  "v_add_u32 v244, 0x8000, v244\n\t" \
  "v_add_u32 v245, 0x8000, v245\n\t" \
  "v_add_u32 v246, 0x8000, v246\n\t" \
  "v_add_u32 v247, 0x8000, v247\n\t" \
  "s_waitcnt lgkmcnt(10)\n\t" \
  "v_mfma_f32_16x16x32_bf16 v[0:3], v[160:163], v[176:179], 0\n\t" \
  "ds_read_b64_tr_b16 v[232:233], v246 offset:0\n\t" \
  "v_mfma_f32_16x16x32_bf16 v[4:7], v[164:167], v[176:179], 0\n\t" \
  "ds_read_b64_tr_b16 v[234:235], v246 offset:1024\n\t" \
  "v_mfma_f32_16x16x32_bf16 v[8:11], v[168:171], v[176:179], 0\n\t" \
  "ds_read_b128 v[216:219], v244 offset:0\n\t" \
  "v_mfma_f32_16x16x32_bf16 v[12:15], v[172:175], v[176:179], 0\n\t" \
  "ds_read_b128 v[220:223], v244 offset:2048\n\t" \
  "v_mfma_f32_16x16x32_bf16 v[16:19], v[160:163], v[180:183], 0\n\t" \
  "ds_read_b128 v[224:227], v244 offset:4096\n\t" \
  "v_mfma_f32_16x16x32_bf16 v[20:23], v[164:167], v[180:183], 0\n\t" \
  "v_mfma_f32_16x16x32_bf16 v[24:27], v[168:171], v[180:183], 0\n\t" \
  "ds_read_b128 v[228:231], v244 offset:6144\n\t" \
  "v_mfma_f32_16x16x32_bf16 v[28:31], v[172:175], v[180:183], 0\n\t" \
  "ds_read_b64_tr_b16 v[236:237], v247 offset:0\n\t" \
  "v_mfma_f32_16x16x32_bf16 v[32:35], v[160:163], v[184:187], 0\n\t" \
  "ds_read_b64_tr_b16 v[238:239], v247 offset:1024\n\t" \
  "v_mfma_f32_16x16x32_bf16 v[36:39], v[164:167], v[184:187], 0\n\t" \
  "ds_read_b64_tr_b16 v[240:241], v252 offset:8192\n\t" \
  "v_mfma_f32_16x16x32_bf16 v[40:43], v[168:171], v[184:187], 0\n\t" \
  "ds_read_b64_tr_b16 v[242:243], v252 offset:8704\n\t" \
  "v_mfma_f32_16x16x32_bf16 v[44:47], v[172:175], v[184:187], 0\n\t" \
  "s_waitcnt vmcnt(0)\n\t" \
  "s_mov_b64 exec, 0xff\n\t" \
  "buffer_store_dwordx2 v[84:85], v82, %[rw], %[spflag] offen sc1\n\t" \
  "s_mov_b64 exec, -1\n\t" \
  "s_waitcnt lgkmcnt(10)\n\t" \
  "v_mfma_f32_16x16x32_bf16 v[0:3], v[188:191], v[204:207], v[0:3]\n\t" \
  "ds_read_b64_tr_b16 v[176:177], v246 offset:8192\n\t" \
  "v_mfma_f32_16x16x32_bf16 v[4:7], v[192:195], v[204:207], v[4:7]\n\t" \
  "ds_read_b64_tr_b16 v[178:179], v246 offset:9216\n\t" \
  "v_mfma_f32_16x16x32_bf16 v[8:11], v[196:199], v[204:207], v[8:11]\n\t" \
  "ds_read_b128 v[160:163], v245 offset:0\n\t" \
  "v_mfma_f32_16x16x32_bf16 v[12:15], v[200:203], v[204:207], v[12:15]\n\t" \
  "ds_read_b128 v[164:167], v245 offset:2048\n\t" \
  "v_mfma_f32_16x16x32_bf16 v[16:19], v[188:191], v[208:211], v[16:19]\n\t" \
  "ds_read_b128 v[168:171], v245 offset:4096\n\t" \
  "v_mfma_f32_16x16x32_bf16 v[20:23], v[192:195], v[208:211], v[20:23]\n\t" \
  "v_mfma_f32_16x16x32_bf16 v[24:27], v[196:199], v[208:211], v[24:27]\n\t" \
  "ds_read_b128 v[172:175], v245 offset:6144\n\t" \
  "v_mfma_f32_16x16x32_bf16 v[28:31], v[200:203], v[208:211], v[28:31]\n\t" \
  "ds_read_b64_tr_b16 v[180:181], v247 offset:8192\n\t" \
  "v_mfma_f32_16x16x32_bf16 v[32:35], v[188:191], v[212:215], v[32:35]\n\t" \
  "ds_read_b64_tr_b16 v[182:183], v247 offset:9216\n\t" \
  "v_mfma_f32_16x16x32_bf16 v[36:39], v[192:195], v[212:215], v[36:39]\n\t" \
  "ds_read_b64_tr_b16 v[184:185], v252 offset:12288\n\t" \
  "v_mfma_f32_16x16x32_bf16 v[40:43], v[196:199], v[212:215], v[40:43]\n\t" \
  "ds_read_b64_tr_b16 v[186:187], v252 offset:12800\n\t" \
  "v_mfma_f32_16x16x32_bf16 v[44:47], v[200:203], v[212:215], v[44:47]\n\t" \
  "v_add_u32 v244, 0x8000, v244\n\t" \
  "v_add_u32 v245, 0x8000, v245\n\t" \
  "v_add_u32 v246, 0x8000, v246\n\t" \
  "v_add_u32 v247, 0x8000, v247\n\t" \
  "s_waitcnt lgkmcnt(10)\n\t" \
  "s_barrier\n\t" \
  "v_mfma_f32_16x16x32_bf16 v[0:3], v[216:219], v[232:235], v[0:3]\n\t" \
  "ds_read_b64_tr_b16 v[204:205], v246 offset:0\n\t" \
  "v_mfma_f32_16x16x32_bf16 v[4:7], v[220:223], v[232:235], v[4:7]\n\t" \
  "ds_read_b64_tr_b16 v[206:207], v246 offset:1024\n\t" \
  "v_mfma_f32_16x16x32_bf16 v[8:11], v[224:227], v[232:235], v[8:11]\n\t" \
  "ds_read_b128 v[188:191], v244 offset:0\n\t" \
  "v_mfma_f32_16x16x32_bf16 v[12:15], v[228:231], v[232:235], v[12:15]\n\t" \
  "ds_read_b128 v[192:195], v244 offset:2048\n\t" \
  "v_mfma_f32_16x16x32_bf16 v[16:19], v[216:219], v[236:239], v[16:19]\n\t" \
  "ds_read_b128 v[196:199], v244 offset:4096\n\t" \
  "v_mfma_f32_16x16x32_bf16 v[20:23], v[220:223], v[236:239], v[20:23]\n\t" \
  "v_mfma_f32_16x16x32_bf16 v[24:27], v[224:227], v[236:239], v[24:27]\n\t" \
  "ds_read_b128 v[200:203], v244 offset:6144\n\t" \
  "v_mfma_f32_16x16x32_bf16 v[28:31], v[228:231], v[236:239], v[28:31]\n\t" \
  "ds_read_b64_tr_b16 v[208:209], v247 offset:0\n\t" \
  "v_mfma_f32_16x16x32_bf16 v[32:35], v[216:219], v[240:243], v[32:35]\n\t" \
  "ds_read_b64_tr_b16 v[210:211], v247 offset:1024\n\t" \
  "v_mfma_f32_16x16x32_bf16 v[36:39], v[220:223], v[240:243], v[36:39]\n\t" \
  "ds_read_b64_tr_b16 v[212:213], v252 offset:16384\n\t" \
  "v_mfma_f32_16x16x32_bf16 v[40:43], v[224:227], v[240:243], v[40:43]\n\t" \
  "ds_read_b64_tr_b16 v[214:215], v252 offset:16896\n\t" \
  "v_mfma_f32_16x16x32_bf16 v[44:47], v[228:231], v[240:243], v[44:47]\n\t" \
  "s_waitcnt lgkmcnt(10)\n\t" \
  "v_mfma_f32_16x16x32_bf16 v[0:3], v[160:163], v[176:179], v[0:3]\n\t" \
  "ds_read_b64_tr_b16 v[232:233], v246 offset:8192\n\t" \
  "v_mfma_f32_16x16x32_bf16 v[4:7], v[164:167], v[176:179], v[4:7]\n\t" \
  "ds_read_b64_tr_b16 v[234:235], v246 offset:9216\n\t" \
  "v_mfma_f32_16x16x32_bf16 v[8:11], v[168:171], v[176:179], v[8:11]\n\t" \
  "ds_read_b128 v[216:219], v245 offset:0\n\t" \
  "v_mfma_f32_16x16x32_bf16 v[12:15], v[172:175], v[176:179], v[12:15]\n\t" \
  "ds_read_b128 v[220:223], v245 offset:2048\n\t" \
  "v_mfma_f32_16x16x32_bf16 v[16:19], v[160:163], v[180:183], v[16:19]\n\t" \
  "ds_read_b128 v[224:227], v245 offset:4096\n\t" \
  "v_cvt_pk_bf16_f32 v48, v0, v1\n\t" \
  "v_cvt_pk_bf16_f32 v49, v2, v3\n\t" \
  "v_mfma_f32_16x16x32_bf16 v[20:23], v[164:167], v[180:183], v[20:23]\n\t" \
  "v_cvt_pk_bf16_f32 v50, v4, v5\n\t" \
  "v_cvt_pk_bf16_f32 v51, v6, v7\n\t" \
  "v_mfma_f32_16x16x32_bf16 v[24:27], v[168:171], v[180:183], v[24:27]\n\t" \
  "ds_read_b128 v[228:231], v245 offset:6144\n\t" \
  "v_cvt_pk_bf16_f32 v52, v8, v9\n\t" \
  "v_cvt_pk_bf16_f32 v53, v10, v11\n\t" \
  "v_mfma_f32_16x16x32_bf16 v[28:31], v[172:175], v[180:183], v[28:31]\n\t" \
  "ds_read_b64_tr_b16 v[236:237], v247 offset:8192\n\t" \
  "v_cvt_pk_bf16_f32 v54, v12, v13\n\t" \
  "v_cvt_pk_bf16_f32 v55, v14, v15\n\t" \
  "v_mfma_f32_16x16x32_bf16 v[32:35], v[160:163], v[184:187], v[32:35]\n\t" \
  "ds_read_b64_tr_b16 v[238:239], v247 offset:9216\n\t" \
  "v_cvt_pk_bf16_f32 v56, v16, v17\n\t" \
  "v_cvt_pk_bf16_f32 v57, v18, v19\n\t" \
  "v_mfma_f32_16x16x32_bf16 v[36:39], v[164:167], v[184:187], v[36:39]\n\t" \
  "ds_read_b64_tr_b16 v[240:241], v252 offset:20480\n\t" \
  "v_cvt_pk_bf16_f32 v58, v20, v21\n\t" \
  "v_cvt_pk_bf16_f32 v59, v22, v23\n\t" \
  "v_mfma_f32_16x16x32_bf16 v[40:43], v[168:171], v[184:187], v[40:43]\n\t" \
  "ds_read_b64_tr_b16 v[242:243], v252 offset:20992\n\t" \
  "v_cvt_pk_bf16_f32 v60, v24, v25\n\t" \
  "v_cvt_pk_bf16_f32 v61, v26, v27\n\t" \
  "v_mfma_f32_16x16x32_bf16 v[44:47], v[172:175], v[184:187], v[44:47]\n\t" \
  "v_cvt_pk_bf16_f32 v62, v28, v29\n\t" \
  "v_cvt_pk_bf16_f32 v63, v30, v31\n\t" \
  "v_add_u32 v244, 0xfffe8000, v244\n\t" \
  "v_add_u32 v245, 0xfffe8000, v245\n\t" \
  "v_add_u32 v246, 0xfffe8000, v246\n\t" \
  "v_add_u32 v247, 0xfffe8000, v247\n\t" \
  "s_waitcnt lgkmcnt(10)\n\t" \
  "s_barrier\n\t" \
  "v_mfma_f32_16x16x32_bf16 v[0:3], v[188:191], v[204:207], 0\n\t" \
  "ds_read_b64_tr_b16 v[176:177], v246 offset:0\n\t" \
  "v_cvt_pk_bf16_f32 v64, v32, v33\n\t" \
  "v_cvt_pk_bf16_f32 v65, v34, v35\n\t" \
  "v_mfma_f32_16x16x32_bf16 v[4:7], v[192:195], v[204:207], 0\n\t" \
  "ds_read_b64_tr_b16 v[178:179], v246 offset:1024\n\t" \
  "v_cvt_pk_bf16_f32 v66, v36, v37\n\t" \
  "v_cvt_pk_bf16_f32 v67, v38, v39\n\t" \
  "v_mfma_f32_16x16x32_bf16 v[8:11], v[196:199], v[204:207], 0\n\t" \
  "ds_read_b128 v[160:163], v244 offset:0\n\t" \
  "v_cvt_pk_bf16_f32 v68, v40, v41\n\t" \
  "v_cvt_pk_bf16_f32 v69, v42, v43\n\t" \
  "v_mfma_f32_16x16x32_bf16 v[12:15], v[200:203], v[204:207], 0\n\t" \
  "ds_read_b128 v[164:167], v244 offset:2048\n\t" \
  "v_cvt_pk_bf16_f32 v70, v44, v45\n\t" \
  "v_cvt_pk_bf16_f32 v71, v46, v47\n\t" \
  "v_mfma_f32_16x16x32_bf16 v[16:19], v[188:191], v[208:211], 0\n\t" \
  "ds_read_b128 v[168:171], v244 offset:4096\n\t" \
  "v_mfma_f32_16x16x32_bf16 v[20:23], v[192:195], v[208:211], 0\n\t" \
  "v_mfma_f32_16x16x32_bf16 v[24:27], v[196:199], v[208:211], 0\n\t" \
  "ds_read_b128 v[172:175], v244 offset:6144\n\t" \
  "v_mfma_f32_16x16x32_bf16 v[28:31], v[200:203], v[208:211], 0\n\t" \
  "ds_read_b64_tr_b16 v[180:181], v247 offset:0\n\t" \
  "v_mfma_f32_16x16x32_bf16 v[32:35], v[188:191], v[212:215], 0\n\t" \
  "ds_read_b64_tr_b16 v[182:183], v247 offset:1024\n\t" \
  "v_mfma_f32_16x16x32_bf16 v[36:39], v[192:195], v[212:215], 0\n\t" \
  "ds_read_b64_tr_b16 v[184:185], v252 offset:24576\n\t" \
  "v_mfma_f32_16x16x32_bf16 v[40:43], v[196:199], v[212:215], 0\n\t" \
  "ds_read_b64_tr_b16 v[186:187], v252 offset:25088\n\t" \
  "v_mfma_f32_16x16x32_bf16 v[44:47], v[200:203], v[212:215], 0\n\t" \
  "s_waitcnt lgkmcnt(10)\n\t" \
  "v_mfma_f32_16x16x32_bf16 v[0:3], v[216:219], v[232:235], v[0:3]\n\t" \
  "ds_read_b64_tr_b16 v[204:205], v246 offset:8192\n\t" \
  "v_mfma_f32_16x16x32_bf16 v[4:7], v[220:223], v[232:235], v[4:7]\n\t" \
  "ds_read_b64_tr_b16 v[206:207], v246 offset:9216\n\t" \
  "v_mfma_f32_16x16x32_bf16 v[8:11], v[224:227], v[232:235], v[8:11]\n\t" \
  "ds_read_b128 v[188:191], v245 offset:0\n\t" \
  "v_mfma_f32_16x16x32_bf16 v[12:15], v[228:231], v[232:235], v[12:15]\n\t" \
  "ds_read_b128 v[192:195], v245 offset:2048\n\t" \
  "v_mfma_f32_16x16x32_bf16 v[16:19], v[216:219], v[236:239], v[16:19]\n\t" \
  "ds_read_b128 v[196:199], v245 offset:4096\n\t" \
  "v_mfma_f32_16x16x32_bf16 v[20:23], v[220:223], v[236:239], v[20:23]\n\t" \
  "v_mfma_f32_16x16x32_bf16 v[24:27], v[224:227], v[236:239], v[24:27]\n\t" \
  "ds_read_b128 v[200:203], v245 offset:6144\n\t" \
  "v_mfma_f32_16x16x32_bf16 v[28:31], v[228:231], v[236:239], v[28:31]\n\t" \
  "ds_read_b64_tr_b16 v[208:209], v247 offset:8192\n\t" \
  "v_mfma_f32_16x16x32_bf16 v[32:35], v[216:219], v[240:243], v[32:35]\n\t" \
  "ds_read_b64_tr_b16 v[210:211], v247 offset:9216\n\t" \
  "v_mfma_f32_16x16x32_bf16 v[36:39], v[220:223], v[240:243], v[36:39]\n\t" \
  "ds_read_b64_tr_b16 v[212:213], v252 offset:28672\n\t" \
  "v_mfma_f32_16x16x32_bf16 v[40:43], v[224:227], v[240:243], v[40:43]\n\t" \
  "ds_read_b64_tr_b16 v[214:215], v252 offset:29184\n\t" \
  "v_mfma_f32_16x16x32_bf16 v[44:47], v[228:231], v[240:243], v[44:47]\n\t" \
  "v_add_u32 v244, 0x6000, v244\n\t" \
  "v_add_u32 v245, 0x6000, v245\n\t" \
  "v_add_u32 v246, 0xc000, v246\n\t" \
  "v_add_u32 v247, 0xc000, v247\n\t" \
  "s_waitcnt lgkmcnt(10)\n\t" \
  "s_barrier\n\t" \
  "v_mfma_f32_16x16x32_bf16 v[0:3], v[160:163], v[176:179], v[0:3]\n\t" \
  "ds_read_b64_tr_b16 v[232:233], v246 offset:0\n\t" \
  "v_mfma_f32_16x16x32_bf16 v[4:7], v[164:167], v[176:179], v[4:7]\n\t" \
  "ds_read_b64_tr_b16 v[234:235], v246 offset:1024\n\t" \
  "v_mfma_f32_16x16x32_bf16 v[8:11], v[168:171], v[176:179], v[8:11]\n\t" \
  "v_mfma_f32_16x16x32_bf16 v[12:15], v[172:175], v[176:179], v[12:15]\n\t" \
  "ds_read_b128 v[216:219], v244 offset:0\n\t" \
  "v_mfma_f32_16x16x32_bf16 v[16:19], v[160:163], v[180:183], v[16:19]\n\t" \
  "ds_read_b128 v[220:223], v244 offset:2048\n\t" \
  "v_mfma_f32_16x16x32_bf16 v[20:23], v[164:167], v[180:183], v[20:23]\n\t" \
  "v_mfma_f32_16x16x32_bf16 v[24:27], v[168:171], v[180:183], v[24:27]\n\t" \
  "ds_read_b128 v[224:227], v244 offset:4096\n\t" \
  "v_mfma_f32_16x16x32_bf16 v[28:31], v[172:175], v[180:183], v[28:31]\n\t" \
  "ds_read_b128 v[228:231], v244 offset:6144\n\t" \
  "v_mfma_f32_16x16x32_bf16 v[32:35], v[160:163], v[184:187], v[32:35]\n\t" \
  "v_mfma_f32_16x16x32_bf16 v[36:39], v[164:167], v[184:187], v[36:39]\n\t" \
  "ds_read_b64_tr_b16 v[236:237], v247 offset:0\n\t" \
  "v_mfma_f32_16x16x32_bf16 v[40:43], v[168:171], v[184:187], v[40:43]\n\t" \
  "ds_read_b64_tr_b16 v[238:239], v247 offset:1024\n\t" \
  "v_mfma_f32_16x16x32_bf16 v[44:47], v[172:175], v[184:187], v[44:47]\n\t" \
  "s_waitcnt lgkmcnt(8)\n\t" \
  "v_mfma_f32_16x16x32_bf16 v[0:3], v[188:191], v[204:207], v[0:3]\n\t" \
  "ds_read_b64_tr_b16 v[176:177], v246 offset:32768\n\t" \
  "v_mfma_f32_16x16x32_bf16 v[4:7], v[192:195], v[204:207], v[4:7]\n\t" \
  "ds_read_b64_tr_b16 v[178:179], v246 offset:33792\n\t" \
  "v_mfma_f32_16x16x32_bf16 v[8:11], v[196:199], v[204:207], v[8:11]\n\t" \
  "v_mfma_f32_16x16x32_bf16 v[12:15], v[200:203], v[204:207], v[12:15]\n\t" \
  "ds_read_b128 v[160:163], v245 offset:0\n\t" \
  "v_mfma_f32_16x16x32_bf16 v[16:19], v[188:191], v[208:211], v[16:19]\n\t" \
  "ds_read_b128 v[164:167], v245 offset:2048\n\t" \
  "v_cvt_pk_bf16_f32 v72, v0, v1\n\t" \
  "v_cvt_pk_bf16_f32 v73, v2, v3\n\t" \
  "v_mfma_f32_16x16x32_bf16 v[20:23], v[192:195], v[208:211], v[20:23]\n\t" \
  "v_cvt_pk_bf16_f32 v74, v4, v5\n\t" \
  "v_cvt_pk_bf16_f32 v75, v6, v7\n\t" \
  "v_mfma_f32_16x16x32_bf16 v[24:27], v[196:199], v[208:211], v[24:27]\n\t" \
  "ds_read_b128 v[168:171], v245 offset:4096\n\t" \
  "v_cvt_pk_bf16_f32 v76, v8, v9\n\t" \
  "v_cvt_pk_bf16_f32 v77, v10, v11\n\t" \
  "v_mfma_f32_16x16x32_bf16 v[28:31], v[200:203], v[208:211], v[28:31]\n\t" \
  "ds_read_b128 v[172:175], v245 offset:6144\n\t" \
  "v_cvt_pk_bf16_f32 v78, v12, v13\n\t" \
  "v_cvt_pk_bf16_f32 v79, v14, v15\n\t" \
  "v_mfma_f32_16x16x32_bf16 v[32:35], v[188:191], v[212:215], v[32:35]\n\t" \
  "v_cvt_pk_bf16_f32 v80, v16, v17\n\t" \
  "v_cvt_pk_bf16_f32 v81, v18, v19\n\t" \
  "v_mfma_f32_16x16x32_bf16 v[36:39], v[192:195], v[212:215], v[36:39]\n\t" \
  "ds_read_b64_tr_b16 v[180:181], v247 offset:32768\n\t" \
  "v_cvt_pk_bf16_f32 v82, v20, v21\n\t" \
  "v_cvt_pk_bf16_f32 v83, v22, v23\n\t" \
  "v_mfma_f32_16x16x32_bf16 v[40:43], v[196:199], v[212:215], v[40:43]\n\t" \
  "ds_read_b64_tr_b16 v[182:183], v247 offset:33792\n\t" \
  "v_cvt_pk_bf16_f32 v84, v24, v25\n\t" \
  "v_cvt_pk_bf16_f32 v85, v26, v27\n\t" \
  "v_mfma_f32_16x16x32_bf16 v[44:47], v[200:203], v[212:215], v[44:47]\n\t" \
  "v_cvt_pk_bf16_f32 v86, v28, v29\n\t" \
  "v_cvt_pk_bf16_f32 v87, v30, v31\n\t" \
  "v_add_u32 v244, 0x2000, v244\n\t" \
  "v_add_u32 v245, 0x2000, v245\n\t" \
  "v_add_u32 v246, 0xffffc000, v246\n\t" \
  "v_add_u32 v247, 0xffffc000, v247\n\t" \
  "s_waitcnt lgkmcnt(8)\n\t" \
  "s_barrier\n\t" \
  "v_mfma_f32_16x16x32_bf16 v[0:3], v[216:219], v[232:235], 0\n\t" \
  "ds_read_b64_tr_b16 v[204:205], v246 offset:0\n\t" \
  "v_cvt_pk_bf16_f32 v88, v32, v33\n\t" \
  "v_cvt_pk_bf16_f32 v89, v34, v35\n\t" \
  "v_mfma_f32_16x16x32_bf16 v[4:7], v[220:223], v[232:235], 0\n\t" \
  "ds_read_b64_tr_b16 v[206:207], v246 offset:1024\n\t" \
  "v_cvt_pk_bf16_f32 v90, v36, v37\n\t" \
  "v_cvt_pk_bf16_f32 v91, v38, v39\n\t" \
  "v_mfma_f32_16x16x32_bf16 v[8:11], v[224:227], v[232:235], 0\n\t" \
  "ds_read_b128 v[188:191], v244 offset:0\n\t" \
  "v_cvt_pk_bf16_f32 v92, v40, v41\n\t" \
  "v_cvt_pk_bf16_f32 v93, v42, v43\n\t" \
  "v_mfma_f32_16x16x32_bf16 v[12:15], v[228:231], v[232:235], 0\n\t" \
  "ds_read_b128 v[192:195], v244 offset:2048\n\t" \
  "v_cvt_pk_bf16_f32 v94, v44, v45\n\t" \
  "v_cvt_pk_bf16_f32 v95, v46, v47\n\t" \
  "v_mfma_f32_16x16x32_bf16 v[16:19], v[216:219], v[236:239], 0\n\t" \
  "ds_read_b128 v[196:199], v244 offset:4096\n\t" \
  "v_mfma_f32_16x16x32_bf16 v[20:23], v[220:223], v[236:239], 0\n\t" \
  "ds_read_b128 v[200:203], v244 offset:6144\n\t" \
  "v_mfma_f32_16x16x32_bf16 v[24:27], v[224:227], v[236:239], 0\n\t" \
  "ds_read_b64_tr_b16 v[208:209], v247 offset:0\n\t" \
  "v_mfma_f32_16x16x32_bf16 v[28:31], v[228:231], v[236:239], 0\n\t" \
  "ds_read_b64_tr_b16 v[210:211], v247 offset:1024\n\t" \
  "s_waitcnt lgkmcnt(8)\n\t" \
  "v_mfma_f32_16x16x32_bf16 v[0:3], v[160:163], v[176:179], v[0:3]\n\t" \
  "ds_read_b64_tr_b16 v[232:233], v246 offset:8192\n\t" \
  "v_mfma_f32_16x16x32_bf16 v[4:7], v[164:167], v[176:179], v[4:7]\n\t" \
  "ds_read_b64_tr_b16 v[234:235], v246 offset:9216\n\t" \
  "v_mfma_f32_16x16x32_bf16 v[8:11], v[168:171], v[176:179], v[8:11]\n\t" \
  "ds_read_b128 v[216:219], v245 offset:0\n\t" \
  "v_mfma_f32_16x16x32_bf16 v[12:15], v[172:175], v[176:179], v[12:15]\n\t" \
  "ds_read_b128 v[220:223], v245 offset:2048\n\t" \
  "v_mfma_f32_16x16x32_bf16 v[16:19], v[160:163], v[180:183], v[16:19]\n\t" \
  "ds_read_b128 v[224:227], v245 offset:4096\n\t" \
  "v_mfma_f32_16x16x32_bf16 v[20:23], v[164:167], v[180:183], v[20:23]\n\t" \
  "ds_read_b128 v[228:231], v245 offset:6144\n\t" \
  "v_mfma_f32_16x16x32_bf16 v[24:27], v[168:171], v[180:183], v[24:27]\n\t" \
  "ds_read_b64_tr_b16 v[236:237], v247 offset:8192\n\t" \
  "v_mfma_f32_16x16x32_bf16 v[28:31], v[172:175], v[180:183], v[28:31]\n\t" \
  "ds_read_b64_tr_b16 v[238:239], v247 offset:9216\n\t" \
  "v_add_u32 v244, 0x8000, v244\n\t" \
  "v_add_u32 v245, 0x8000, v245\n\t" \
  "v_add_u32 v246, 0x8000, v246\n\t" \
  "v_add_u32 v247, 0x8000, v247\n\t" \
  "s_waitcnt lgkmcnt(8)\n\t" \
  "s_barrier\n\t" \
  "v_mfma_f32_16x16x32_bf16 v[0:3], v[188:191], v[204:207], v[0:3]\n\t" \
  "ds_read_b64_tr_b16 v[176:177], v246 offset:0\n\t" \
  "v_mfma_f32_16x16x32_bf16 v[4:7], v[192:195], v[204:207], v[4:7]\n\t" \
  "ds_read_b64_tr_b16 v[178:179], v246 offset:1024\n\t" \
  "v_mfma_f32_16x16x32_bf16 v[8:11], v[196:199], v[204:207], v[8:11]\n\t" \
  "ds_read_b128 v[160:163], v244 offset:0\n\t" \
  "v_mfma_f32_16x16x32_bf16 v[12:15], v[200:203], v[204:207], v[12:15]\n\t" \
  "ds_read_b128 v[164:167], v244 offset:2048\n\t" \
  "v_mfma_f32_16x16x32_bf16 v[16:19], v[188:191], v[208:211], v[16:19]\n\t" \
  "ds_read_b128 v[168:171], v244 offset:4096\n\t" \
  "v_mfma_f32_16x16x32_bf16 v[20:23], v[192:195], v[208:211], v[20:23]\n\t" \
  "ds_read_b128 v[172:175], v244 offset:6144\n\t" \
  "v_mfma_f32_16x16x32_bf16 v[24:27], v[196:199], v[208:211], v[24:27]\n\t" \
  "ds_read_b64_tr_b16 v[180:181], v247 offset:0\n\t" \
  "v_mfma_f32_16x16x32_bf16 v[28:31], v[200:203], v[208:211], v[28:31]\n\t" \
  "ds_read_b64_tr_b16 v[182:183], v247 offset:1024\n\t" \
  "s_waitcnt lgkmcnt(8)\n\t" \
  "v_mfma_f32_16x16x32_bf16 v[0:3], v[216:219], v[232:235], v[0:3]\n\t" \
  "ds_read_b64_tr_b16 v[204:205], v246 offset:8192\n\t" \
  "v_mfma_f32_16x16x32_bf16 v[4:7], v[220:223], v[232:235], v[4:7]\n\t" \
  "ds_read_b64_tr_b16 v[206:207], v246 offset:9216\n\t" \
  "v_mfma_f32_16x16x32_bf16 v[8:11], v[224:227], v[232:235], v[8:11]\n\t" \
  "ds_read_b128 v[188:191], v245 offset:0\n\t" \
  "v_mfma_f32_16x16x32_bf16 v[12:15], v[228:231], v[232:235], v[12:15]\n\t" \
  "ds_read_b128 v[192:195], v245 offset:2048\n\t" \
  "v_mfma_f32_16x16x32_bf16 v[16:19], v[216:219], v[236:239], v[16:19]\n\t" \
  "ds_read_b128 v[196:199], v245 offset:4096\n\t" \
  "v_cvt_pk_bf16_f32 v96, v0, v1\n\t" \
  "v_cvt_pk_bf16_f32 v97, v2, v3\n\t" \
  "v_mfma_f32_16x16x32_bf16 v[20:23], v[220:223], v[236:239], v[20:23]\n\t" \
  "ds_read_b128 v[200:203], v245 offset:6144\n\t" \
  "v_cvt_pk_bf16_f32 v98, v4, v5\n\t" \
  "v_cvt_pk_bf16_f32 v99, v6, v7\n\t" \
  "v_mfma_f32_16x16x32_bf16 v[24:27], v[224:227], v[236:239], v[24:27]\n\t" \
  "ds_read_b64_tr_b16 v[208:209], v247 offset:8192\n\t" \
  "v_cvt_pk_bf16_f32 v100, v8, v9\n\t" \
  "v_cvt_pk_bf16_f32 v101, v10, v11\n\t" \
  "v_mfma_f32_16x16x32_bf16 v[28:31], v[228:231], v[236:239], v[28:31]\n\t" \
  "ds_read_b64_tr_b16 v[210:211], v247 offset:9216\n\t" \
  "v_cvt_pk_bf16_f32 v102, v12, v13\n\t" \
  "v_cvt_pk_bf16_f32 v103, v14, v15\n\t" \
  "v_add_u32 v244, 0x8000, v244\n\t" \
  "v_add_u32 v245, 0x8000, v245\n\t" \
  "v_add_u32 v246, 0x8000, v246\n\t" \
  "v_add_u32 v247, 0x8000, v247\n\t" \
  "s_waitcnt lgkmcnt(8)\n\t" \
  "s_barrier\n\t" \
  "v_mfma_f32_16x16x32_bf16 v[0:3], v[160:163], v[176:179], 0\n\t" \
  "ds_read_b64_tr_b16 v[232:233], v246 offset:0\n\t" \
  "v_cvt_pk_bf16_f32 v104, v16, v17\n\t" \
  "v_cvt_pk_bf16_f32 v105, v18, v19\n\t" \
  "v_mfma_f32_16x16x32_bf16 v[4:7], v[164:167], v[176:179], 0\n\t" \
  "ds_read_b64_tr_b16 v[234:235], v246 offset:1024\n\t" \
  "v_cvt_pk_bf16_f32 v106, v20, v21\n\t" \
  "v_cvt_pk_bf16_f32 v107, v22, v23\n\t" \
  "v_mfma_f32_16x16x32_bf16 v[8:11], v[168:171], v[176:179], 0\n\t" \
  "ds_read_b128 v[216:219], v244 offset:0\n\t" \
  "v_cvt_pk_bf16_f32 v108, v24, v25\n\t" \
  "v_cvt_pk_bf16_f32 v109, v26, v27\n\t" \
  "v_mfma_f32_16x16x32_bf16 v[12:15], v[172:175], v[176:179], 0\n\t" \
  "ds_read_b128 v[220:223], v244 offset:2048\n\t" \
  "v_cvt_pk_bf16_f32 v110, v28, v29\n\t" \
  "v_cvt_pk_bf16_f32 v111, v30, v31\n\t" \
  "v_mfma_f32_16x16x32_bf16 v[16:19], v[160:163], v[180:183], 0\n\t" \
  "ds_read_b128 v[224:227], v244 offset:4096\n\t" \
  "v_mfma_f32_16x16x32_bf16 v[20:23], v[164:167], v[180:183], 0\n\t" \
  "ds_read_b128 v[228:231], v244 offset:6144\n\t" \
  "v_mfma_f32_16x16x32_bf16 v[24:27], v[168:171], v[180:183], 0\n\t" \
  "ds_read_b64_tr_b16 v[236:237], v247 offset:0\n\t" \
  "v_mfma_f32_16x16x32_bf16 v[28:31], v[172:175], v[180:183], 0\n\t" \
  "ds_read_b64_tr_b16 v[238:239], v247 offset:1024\n\t" \
  "s_waitcnt lgkmcnt(8)\n\t" \
  "v_mfma_f32_16x16x32_bf16 v[0:3], v[188:191], v[204:207], v[0:3]\n\t" \
  "ds_read_b64_tr_b16 v[176:177], v246 offset:8192\n\t" \
  "v_mfma_f32_16x16x32_bf16 v[4:7], v[192:195], v[204:207], v[4:7]\n\t" \
  "ds_read_b64_tr_b16 v[178:179], v246 offset:9216\n\t" \
  "v_mfma_f32_16x16x32_bf16 v[8:11], v[196:199], v[204:207], v[8:11]\n\t" \
  "ds_read_b128 v[160:163], v245 offset:0\n\t" \
  "v_mfma_f32_16x16x32_bf16 v[12:15], v[200:203], v[204:207], v[12:15]\n\t" \
  "ds_read_b128 v[164:167], v245 offset:2048\n\t" \
  "v_mfma_f32_16x16x32_bf16 v[16:19], v[188:191], v[208:211], v[16:19]\n\t" \
  "ds_read_b128 v[168:171], v245 offset:4096\n\t" \
  "v_mfma_f32_16x16x32_bf16 v[20:23], v[192:195], v[208:211], v[20:23]\n\t" \
  "ds_read_b128 v[172:175], v245 offset:6144\n\t" \
  "v_mfma_f32_16x16x32_bf16 v[24:27], v[196:199], v[208:211], v[24:27]\n\t" \
  "ds_read_b64_tr_b16 v[180:181], v247 offset:8192\n\t" \
  "v_mfma_f32_16x16x32_bf16 v[28:31], v[200:203], v[208:211], v[28:31]\n\t" \
  "ds_read_b64_tr_b16 v[182:183], v247 offset:9216\n\t" \
  "v_add_u32 v244, 0xfffe8000, v244\n\t" \
  "v_add_u32 v245, 0xfffe8000, v245\n\t" \
  "v_add_u32 v246, 0xfffe8000, v246\n\t" \
  "v_add_u32 v247, 0xfffe8000, v247\n\t" \
  "s_waitcnt lgkmcnt(8)\n\t" \
  "s_barrier\n\t" \
  "v_mfma_f32_16x16x32_bf16 v[0:3], v[216:219], v[232:235], v[0:3]\n\t" \
  "ds_read_b64_tr_b16 v[204:205], v246 offset:0\n\t" \
  "s_bfe_u32 %[t0], %[or2], 0x30000\n\t" \
  "v_mfma_f32_16x16x32_bf16 v[4:7], v[220:223], v[232:235], v[4:7]\n\t" \
  "ds_read_b64_tr_b16 v[206:207], v246 offset:1024\n\t" \
  "v_mfma_f32_16x16x32_bf16 v[8:11], v[224:227], v[232:235], v[8:11]\n\t" \
  "ds_read_b128 v[188:191], v244 offset:0\n\t" \
  "v_lshl_add_u32 v248, %[t0], 12, v253\n\t" \
  "v_mfma_f32_16x16x32_bf16 v[12:15], v[228:231], v[232:235], v[12:15]\n\t" \
  "ds_read_b128 v[192:195], v244 offset:2048\n\t" \
  "v_mfma_f32_16x16x32_bf16 v[16:19], v[216:219], v[236:239], v[16:19]\n\t" \
  "ds_read_b128 v[196:199], v244 offset:4096\n\t" \
  "s_bfe_u32 %[t0], %[or2], 0x30004\n\t" \
  "v_mfma_f32_16x16x32_bf16 v[20:23], v[220:223], v[236:239], v[20:23]\n\t" \
  "ds_read_b128 v[200:203], v244 offset:6144\n\t" \
  "v_mfma_f32_16x16x32_bf16 v[24:27], v[224:227], v[236:239], v[24:27]\n\t" \
  "ds_read_b64_tr_b16 v[208:209], v247 offset:0\n\t" \
  "v_lshl_add_u32 v249, %[t0], 12, v253\n\t" \
  "v_mfma_f32_16x16x32_bf16 v[28:31], v[228:231], v[236:239], v[28:31]\n\t" \
  "ds_read_b64_tr_b16 v[210:211], v247 offset:1024\n\t" \
  "s_waitcnt lgkmcnt(8)\n\t" \
  "v_mfma_f32_16x16x32_bf16 v[0:3], v[160:163], v[176:179], v[0:3]\n\t" \
  "ds_read_b64_tr_b16 v[232:233], v246 offset:8192\n\t" \
  "ds_read_b64_tr_b16 v[234:235], v246 offset:9216\n\t" \
  "v_mfma_f32_16x16x32_bf16 v[4:7], v[164:167], v[176:179], v[4:7]\n\t" \
  "ds_read_b128 v[216:219], v245 offset:0\n\t" \
  "v_mfma_f32_16x16x32_bf16 v[8:11], v[168:171], v[176:179], v[8:11]\n\t" \
  "ds_read_b128 v[220:223], v245 offset:2048\n\t" \
  "ds_read_b128 v[32:35], v248 offset:0\n\t" \
  "v_mfma_f32_16x16x32_bf16 v[12:15], v[172:175], v[176:179], v[12:15]\n\t" \
  "ds_read_b128 v[36:39], v248 offset:1024\n\t" \
  "v_mfma_f32_16x16x32_bf16 v[16:19], v[160:163], v[180:183], v[16:19]\n\t" \
  "ds_read_b128 v[40:43], v248 offset:2048\n\t" \
  "ds_read_b128 v[44:47], v248 offset:3072\n\t" \
  "v_cvt_pk_bf16_f32 v112, v0, v1\n\t" \
  "v_cvt_pk_bf16_f32 v113, v2, v3\n\t" \
  "v_mfma_f32_16x16x32_bf16 v[20:23], v[164:167], v[180:183], v[20:23]\n\t" \
  "ds_read_b128 v[224:227], v245 offset:4096\n\t" \
  "v_cvt_pk_bf16_f32 v114, v4, v5\n\t" \
  "v_cvt_pk_bf16_f32 v115, v6, v7\n\t" \
  "v_mfma_f32_16x16x32_bf16 v[24:27], v[168:171], v[180:183], v[24:27]\n\t" \
  "ds_read_b128 v[228:231], v245 offset:6144\n\t" \
  "ds_read_b64_tr_b16 v[236:237], v247 offset:8192\n\t" \
  "v_cvt_pk_bf16_f32 v116, v8, v9\n\t" \
  "v_cvt_pk_bf16_f32 v117, v10, v11\n\t" \
  "v_mfma_f32_16x16x32_bf16 v[28:31], v[172:175], v[180:183], v[28:31]\n\t" \
  "ds_read_b64_tr_b16 v[238:239], v247 offset:9216\n\t" \
  "v_cvt_pk_bf16_f32 v118, v12, v13\n\t" \
  "v_cvt_pk_bf16_f32 v119, v14, v15\n\t" \
  "v_add_u32 v244, 0x6000, v244\n\t" \
  "v_add_u32 v245, 0x6000, v245\n\t" \
  "v_add_u32 v246, 0xc000, v246\n\t" \
  "v_add_u32 v247, 0xc000, v247\n\t" \
  "ds_read_b128 v[0:3], v249 offset:0\n\t" \
  "ds_read_b128 v[4:7], v249 offset:1024\n\t" \
  "v_cvt_pk_bf16_f32 v120, v16, v17\n\t" \
  "v_cvt_pk_bf16_f32 v121, v18, v19\n\t" \
  "v_cvt_pk_bf16_f32 v122, v20, v21\n\t" \
  "v_cvt_pk_bf16_f32 v123, v22, v23\n\t" \
  "ds_read_b128 v[8:11], v249 offset:2048\n\t" \
  "ds_read_b128 v[12:15], v249 offset:3072\n\t" \
  "v_cvt_pk_bf16_f32 v124, v24, v25\n\t" \
  "v_cvt_pk_bf16_f32 v125, v26, v27\n\t" \
  "v_cvt_pk_bf16_f32 v126, v28, v29\n\t" \
  "v_cvt_pk_bf16_f32 v127, v30, v31\n\t" \
  "s_waitcnt lgkmcnt(8)\n\t" \
  "v_mfma_f32_16x16x32_bf16 v[128:131], v[48:51], v[32:35], 0\n\t" \
  "s_bfe_u32 %[t0], %[or2], 0x30008\n\t" \
  "v_lshl_add_u32 v248, %[t0], 12, v253\n\t" \
  "v_mfma_f32_16x16x32_bf16 v[136:139], v[48:51], v[36:39], 0\n\t" \
  "s_bfe_u32 %[t0], %[or2], 0x3000c\n\t" \
  "v_lshl_add_u32 v249, %[t0], 12, v253\n\t" \
  "v_mfma_f32_16x16x32_bf16 v[144:147], v[52:55], v[40:43], 0\n\t" \
  "v_mfma_f32_16x16x32_bf16 v[152:155], v[52:55], v[44:47], 0\n\t" \
  "v_mfma_f32_16x16x32_bf16 v[132:135], v[56:59], v[32:35], 0\n\t" \
  "v_mfma_f32_16x16x32_bf16 v[140:143], v[56:59], v[36:39], 0\n\t" \
  "v_mfma_f32_16x16x32_bf16 v[148:151], v[60:63], v[40:43], 0\n\t" \
  "v_mfma_f32_16x16x32_bf16 v[156:159], v[60:63], v[44:47], 0\n\t" \
  "v_mfma_f32_16x16x32_bf16 v[16:19], v[64:67], v[32:35], 0\n\t" \
  "v_mfma_f32_16x16x32_bf16 v[20:23], v[64:67], v[36:39], 0\n\t" \
  "v_mfma_f32_16x16x32_bf16 v[24:27], v[68:71], v[40:43], 0\n\t" \
  "v_mfma_f32_16x16x32_bf16 v[28:31], v[68:71], v[44:47], 0\n\t" \
  "s_waitcnt lgkmcnt(0)\n\t" \
  "v_mfma_f32_16x16x32_bf16 v[128:131], v[72:75], v[0:3], v[128:131]\n\t" \
  "v_mfma_f32_16x16x32_bf16 v[136:139], v[72:75], v[4:7], v[136:139]\n\t" \
  "v_mfma_f32_16x16x32_bf16 v[144:147], v[76:79], v[8:11], v[144:147]\n\t" \
  "ds_read_b128 v[48:51], v248 offset:0\n\t" \
  "v_mfma_f32_16x16x32_bf16 v[152:155], v[76:79], v[12:15], v[152:155]\n\t" \
  "ds_read_b128 v[52:55], v248 offset:1024\n\t" \
  "v_mfma_f32_16x16x32_bf16 v[132:135], v[80:83], v[0:3], v[132:135]\n\t" \
  "ds_read_b128 v[56:59], v248 offset:2048\n\t" \
  "v_mfma_f32_16x16x32_bf16 v[140:143], v[80:83], v[4:7], v[140:143]\n\t" \
  "ds_read_b128 v[60:63], v248 offset:3072\n\t" \
  "v_mfma_f32_16x16x32_bf16 v[148:151], v[84:87], v[8:11], v[148:151]\n\t" \
  "ds_read_b128 v[32:35], v249 offset:0\n\t" \
  "v_mfma_f32_16x16x32_bf16 v[156:159], v[84:87], v[12:15], v[156:159]\n\t" \
  "ds_read_b128 v[36:39], v249 offset:1024\n\t" \
  "v_mfma_f32_16x16x32_bf16 v[16:19], v[88:91], v[0:3], v[16:19]\n\t" \
  "ds_read_b128 v[40:43], v249 offset:2048\n\t" \
  "v_mfma_f32_16x16x32_bf16 v[20:23], v[88:91], v[4:7], v[20:23]\n\t" \
  "ds_read_b128 v[44:47], v249 offset:3072\n\t" \
  "v_mfma_f32_16x16x32_bf16 v[24:27], v[92:95], v[8:11], v[24:27]\n\t" \
  "v_mfma_f32_16x16x32_bf16 v[28:31], v[92:95], v[12:15], v[28:31]\n\t" \
  "s_waitcnt lgkmcnt(4)\n\t" \
  "v_mfma_f32_16x16x32_bf16 v[128:131], v[96:99], v[48:51], v[128:131]\n\t" \
  "v_add_u32 v72, 0xfffe0000, v253\n\t" \
  "v_lshrrev_b32 v72, 1, v72\n\t" \
  "v_mfma_f32_16x16x32_bf16 v[136:139], v[96:99], v[52:55], v[136:139]\n\t" \
  "v_mfma_f32_16x16x32_bf16 v[144:147], v[100:103], v[56:59], v[144:147]\n\t" \
  "v_mfma_f32_16x16x32_bf16 v[152:155], v[100:103], v[60:63], v[152:155]\n\t" \
  "v_cvt_pk_bf16_f32 v16, v16, v17\n\t" \
  "v_cvt_pk_bf16_f32 v17, v18, v19\n\t" \
  "s_xor_b32 %[t0], %[ks], 0\n\t" \
  "s_lshl_b32 %[t0], %[t0], 14\n\t" \
  "s_add_u32 %[t0], %[t0], %[s8]\n\t" \
  "buffer_store_dwordx2 v[16:17], v72, %[rw], %[t0] offen sc1\n\t" \
  "v_mfma_f32_16x16x32_bf16 v[132:135], v[104:107], v[48:51], v[132:135]\n\t" \
  "v_cvt_pk_bf16_f32 v20, v20, v21\n\t" \
  "v_cvt_pk_bf16_f32 v21, v22, v23\n\t" \
  "s_xor_b32 %[t0], %[ks], 1\n\t" \
  "s_lshl_b32 %[t0], %[t0], 14\n\t" \
  "s_add_u32 %[t0], %[t0], %[s8]\n\t" \
  "buffer_store_dwordx2 v[20:21], v72, %[rw], %[t0] offen sc1\n\t" \
  "v_mfma_f32_16x16x32_bf16 v[140:143], v[104:107], v[52:55], v[140:143]\n\t" \
  "v_cvt_pk_bf16_f32 v24, v24, v25\n\t" \
  "v_cvt_pk_bf16_f32 v25, v26, v27\n\t" \
  "s_xor_b32 %[t0], %[ks], 2\n\t" \
  "s_lshl_b32 %[t0], %[t0], 14\n\t" \
  "s_add_u32 %[t0], %[t0], %[s8]\n\t" \
  "buffer_store_dwordx2 v[24:25], v72, %[rw], %[t0] offen sc1\n\t" \
  "v_mfma_f32_16x16x32_bf16 v[148:151], v[108:111], v[56:59], v[148:151]\n\t" \
  "v_cvt_pk_bf16_f32 v28, v28, v29\n\t" \
  "v_cvt_pk_bf16_f32 v29, v30, v31\n\t" \
  "s_xor_b32 %[t0], %[ks], 3\n\t" \
  "s_lshl_b32 %[t0], %[t0], 14\n\t" \
  "s_add_u32 %[t0], %[t0], %[s8]\n\t" \
  "buffer_store_dwordx2 v[28:29], v72, %[rw], %[t0] offen sc1\n\t" \
  "v_mfma_f32_16x16x32_bf16 v[156:159], v[108:111], v[60:63], v[156:159]\n\t" \
  "s_waitcnt lgkmcnt(0)\n\t" \
  "v_mfma_f32_16x16x32_bf16 v[128:131], v[112:115], v[32:35], v[128:131]\n\t" \
  "v_mfma_f32_16x16x32_bf16 v[136:139], v[112:115], v[36:39], v[136:139]\n\t" \
  "v_mov_b32 v48, v72\n\t" \
  "v_lshrrev_b32 v52, 3, v72\n\t" \
  "v_mfma_f32_16x16x32_bf16 v[144:147], v[116:119], v[40:43], v[144:147]\n\t" \
  "v_xor_b32 v49, %[ks], v52\n\t" \
  "v_lshlrev_b32 v52, 7, v52\n\t" \
  "v_mfma_f32_16x16x32_bf16 v[152:155], v[116:119], v[44:47], v[152:155]\n\t" \
  "v_lshl_add_u32 v52, v49, 12, v52\n\t" \
  "v_mfma_f32_16x16x32_bf16 v[132:135], v[120:123], v[32:35], v[132:135]\n\t" \
  "v_mfma_f32_16x16x32_bf16 v[140:143], v[120:123], v[36:39], v[140:143]\n\t" \
  "v_mfma_f32_16x16x32_bf16 v[148:151], v[124:127], v[40:43], v[148:151]\n\t" \
  "v_mfma_f32_16x16x32_bf16 v[156:159], v[124:127], v[44:47], v[156:159]\n\t" \
  "s_barrier\n\t" \
  "v_mfma_f32_16x16x32_bf16 v[0:3], v[188:191], v[204:207], 0\n\t" \
  "ds_read_b64_tr_b16 v[176:177], v246 offset:0\n\t" \
  "v_mfma_f32_16x16x32_bf16 v[4:7], v[192:195], v[204:207], 0\n\t" \
  "ds_read_b64_tr_b16 v[178:179], v246 offset:1024\n\t" \
  "v_mfma_f32_16x16x32_bf16 v[8:11], v[196:199], v[204:207], 0\n\t" \
  "ds_read_b128 v[160:163], v244 offset:0\n\t" \
  "v_mfma_f32_16x16x32_bf16 v[12:15], v[200:203], v[204:207], 0\n\t" \
  "ds_read_b128 v[164:167], v244 offset:2048\n\t" \
  "v_mfma_f32_16x16x32_bf16 v[16:19], v[188:191], v[208:211], 0\n\t" \
  "ds_read_b128 v[168:171], v244 offset:4096\n\t" \
  "v_mfma_f32_16x16x32_bf16 v[20:23], v[192:195], v[208:211], 0\n\t" \
  "ds_read_b128 v[172:175], v244 offset:6144\n\t" \
  "v_mfma_f32_16x16x32_bf16 v[24:27], v[196:199], v[208:211], 0\n\t" \
  "ds_read_b64_tr_b16 v[180:181], v247 offset:0\n\t" \
  "v_mfma_f32_16x16x32_bf16 v[28:31], v[200:203], v[208:211], 0\n\t" \
  "ds_read_b64_tr_b16 v[182:183], v247 offset:1024\n\t" \
  "v_mfma_f32_16x16x32_bf16 v[0:3], v[216:219], v[232:235], v[0:3]\n\t" \
  "ds_read_b64_tr_b16 v[204:205], v246 offset:32768\n\t" \
  "v_mfma_f32_16x16x32_bf16 v[4:7], v[220:223], v[232:235], v[4:7]\n\t" \
  "ds_read_b64_tr_b16 v[206:207], v246 offset:33792\n\t" \
  "v_mfma_f32_16x16x32_bf16 v[8:11], v[224:227], v[232:235], v[8:11]\n\t" \
  "ds_read_b128 v[188:191], v245 offset:0\n\t" \
  "v_mfma_f32_16x16x32_bf16 v[12:15], v[228:231], v[232:235], v[12:15]\n\t" \
  "ds_read_b128 v[192:195], v245 offset:2048\n\t" \
  "v_mfma_f32_16x16x32_bf16 v[16:19], v[216:219], v[236:239], v[16:19]\n\t" \
  "ds_read_b128 v[196:199], v245 offset:4096\n\t" \
  "v_mfma_f32_16x16x32_bf16 v[20:23], v[220:223], v[236:239], v[20:23]\n\t" \
  "ds_read_b128 v[200:203], v245 offset:6144\n\t" \
  "v_mfma_f32_16x16x32_bf16 v[24:27], v[224:227], v[236:239], v[24:27]\n\t" \
  "ds_read_b64_tr_b16 v[208:209], v247 offset:32768\n\t" \
  "v_mfma_f32_16x16x32_bf16 v[28:31], v[228:231], v[236:239], v[28:31]\n\t" \
  "ds_read_b64_tr_b16 v[210:211], v247 offset:33792\n\t" \
  "v_add_u32 v244, 0x2000, v244\n\t" \
  "v_add_u32 v245, 0x2000, v245\n\t" \
  "v_add_u32 v246, 0xffffc000, v246\n\t" \
  "v_add_u32 v247, 0xffffc000, v247\n\t" \
  "s_waitcnt lgkmcnt(8)\n\t" \
  "s_barrier\n\t" \
  "v_mfma_f32_16x16x32_bf16 v[0:3], v[160:163], v[176:179], v[0:3]\n\t" \
  "ds_read_b64_tr_b16 v[232:233], v246 offset:0\n\t" \
  "s_bfe_u32 %[t0], %[or2], 0x30010\n\t" \
  "v_mfma_f32_16x16x32_bf16 v[4:7], v[164:167], v[176:179], v[4:7]\n\t" \
  "ds_read_b64_tr_b16 v[234:235], v246 offset:1024\n\t" \
  "v_mfma_f32_16x16x32_bf16 v[8:11], v[168:171], v[176:179], v[8:11]\n\t" \
  "ds_read_b128 v[216:219], v244 offset:0\n\t" \
  "v_mfma_f32_16x16x32_bf16 v[12:15], v[172:175], v[176:179], v[12:15]\n\t" \
  "ds_read_b128 v[220:223], v244 offset:2048\n\t" \
  "v_mfma_f32_16x16x32_bf16 v[16:19], v[160:163], v[180:183], v[16:19]\n\t" \
  "ds_read_b128 v[224:227], v244 offset:4096\n\t" \
  "v_lshl_add_u32 v248, %[t0], 12, v253\n\t" \
  "v_mfma_f32_16x16x32_bf16 v[20:23], v[164:167], v[180:183], v[20:23]\n\t" \
  "ds_read_b128 v[228:231], v244 offset:6144\n\t" \
  "v_mfma_f32_16x16x32_bf16 v[24:27], v[168:171], v[180:183], v[24:27]\n\t" \
  "ds_read_b64_tr_b16 v[236:237], v247 offset:0\n\t" \
  "v_mfma_f32_16x16x32_bf16 v[28:31], v[172:175], v[180:183], v[28:31]\n\t" \
  "ds_read_b64_tr_b16 v[238:239], v247 offset:1024\n\t" \
  "s_waitcnt lgkmcnt(8)\n\t" \
  "v_mfma_f32_16x16x32_bf16 v[0:3], v[188:191], v[204:207], v[0:3]\n\t" \
  "ds_read_b64_tr_b16 v[176:177], v246 offset:8192\n\t" \
  "ds_read_b64_tr_b16 v[178:179], v246 offset:9216\n\t" \
  "v_mfma_f32_16x16x32_bf16 v[4:7], v[192:195], v[204:207], v[4:7]\n\t" \
  "ds_read_b128 v[160:163], v245 offset:0\n\t" \
  "v_mfma_f32_16x16x32_bf16 v[8:11], v[196:199], v[204:207], v[8:11]\n\t" \
  "ds_read_b128 v[164:167], v245 offset:2048\n\t" \
  "ds_read_b128 v[80:83], v248 offset:0\n\t" \
  "v_mfma_f32_16x16x32_bf16 v[12:15], v[200:203], v[204:207], v[12:15]\n\t" \
  "ds_read_b128 v[84:87], v248 offset:1024\n\t" \
  "v_mfma_f32_16x16x32_bf16 v[16:19], v[188:191], v[208:211], v[16:19]\n\t" \
  "ds_read_b128 v[88:91], v248 offset:2048\n\t" \
  "ds_read_b128 v[92:95], v248 offset:3072\n\t" \
  "v_cvt_pk_bf16_f32 v64, v0, v1\n\t" \
  "v_cvt_pk_bf16_f32 v65, v2, v3\n\t" \
  "v_mfma_f32_16x16x32_bf16 v[20:23], v[192:195], v[208:211], v[20:23]\n\t" \
  "ds_read_b128 v[168:171], v245 offset:4096\n\t" \
  "v_cvt_pk_bf16_f32 v66, v4, v5\n\t" \
  "v_cvt_pk_bf16_f32 v67, v6, v7\n\t" \
  "v_mfma_f32_16x16x32_bf16 v[24:27], v[196:199], v[208:211], v[24:27]\n\t" \
  "ds_read_b128 v[172:175], v245 offset:6144\n\t" \
  "ds_read_b64_tr_b16 v[180:181], v247 offset:8192\n\t" \
  "v_cvt_pk_bf16_f32 v68, v8, v9\n\t" \
  "v_cvt_pk_bf16_f32 v69, v10, v11\n\t" \
  "v_mfma_f32_16x16x32_bf16 v[28:31], v[200:203], v[208:211], v[28:31]\n\t" \
  "ds_read_b64_tr_b16 v[182:183], v247 offset:9216\n\t" \
  "v_cvt_pk_bf16_f32 v70, v12, v13\n\t" \
  "v_cvt_pk_bf16_f32 v71, v14, v15\n\t" \
  "v_add_u32 v244, 0x8000, v244\n\t" \
  "v_add_u32 v245, 0x8000, v245\n\t" \
  "v_add_u32 v246, 0x8000, v246\n\t" \
  "v_add_u32 v247, 0x8000, v247\n\t" \
  "v_lshlrev_b32 v48, 5, v48\n\t" \
  "s_waitcnt vmcnt(0)\n\t" \
  "v_mov_b32 v50, %[tlo]\n\t" \
  "v_mov_b32 v51, %[thi]\n\t" \
  "s_mov_b64 exec, 15\n\t" \
  "buffer_store_dwordx2 v[50:51], v48, %[rw], %[sf8] offen sc1\n\t" \
  "s_mov_b64 exec, -1\n\t" \
  "s_waitcnt lgkmcnt(4)\n\t" \
  "s_barrier\n\t" \
  "v_mfma_f32_16x16x32_bf16 v[128:131], v[64:67], v[80:83], v[128:131]\n\t" \
  "ds_read_b64_tr_b16 v[204:205], v246 offset:0\n\t" \
  "v_cvt_pk_bf16_f32 v72, v16, v17\n\t" \
  "v_cvt_pk_bf16_f32 v73, v18, v19\n\t" \
  "v_mfma_f32_16x16x32_bf16 v[136:139], v[64:67], v[84:87], v[136:139]\n\t" \
  "v_cvt_pk_bf16_f32 v74, v20, v21\n\t" \
  "v_cvt_pk_bf16_f32 v75, v22, v23\n\t" \
  "v_mfma_f32_16x16x32_bf16 v[144:147], v[68:71], v[88:91], v[144:147]\n\t" \
  "ds_read_b64_tr_b16 v[206:207], v246 offset:1024\n\t" \
  "v_cvt_pk_bf16_f32 v76, v24, v25\n\t" \
  "v_cvt_pk_bf16_f32 v77, v26, v27\n\t" \
  "v_mfma_f32_16x16x32_bf16 v[152:155], v[68:71], v[92:95], v[152:155]\n\t" \
  "v_cvt_pk_bf16_f32 v78, v28, v29\n\t" \
  "v_cvt_pk_bf16_f32 v79, v30, v31\n\t" \
  "v_mfma_f32_16x16x32_bf16 v[0:3], v[216:219], v[232:235], 0\n\t" \
  "ds_read_b128 v[188:191], v244 offset:0\n\t" \
  "v_mfma_f32_16x16x32_bf16 v[4:7], v[220:223], v[232:235], 0\n\t" \
  "v_mfma_f32_16x16x32_bf16 v[8:11], v[224:227], v[232:235], 0\n\t" \
  "ds_read_b128 v[192:195], v244 offset:2048\n\t" \
  "v_mfma_f32_16x16x32_bf16 v[12:15], v[228:231], v[232:235], 0\n\t" \
  "v_mfma_f32_16x16x32_bf16 v[132:135], v[72:75], v[80:83], v[132:135]\n\t" \
  "ds_read_b128 v[196:199], v244 offset:4096\n\t" \
  "v_mfma_f32_16x16x32_bf16 v[140:143], v[72:75], v[84:87], v[140:143]\n\t" \
  "v_mfma_f32_16x16x32_bf16 v[148:151], v[76:79], v[88:91], v[148:151]\n\t" \
  "ds_read_b128 v[200:203], v244 offset:6144\n\t" \
  "v_mfma_f32_16x16x32_bf16 v[156:159], v[76:79], v[92:95], v[156:159]\n\t" \
  "v_mfma_f32_16x16x32_bf16 v[16:19], v[216:219], v[236:239], 0\n\t" \
  "ds_read_b64_tr_b16 v[208:209], v247 offset:0\n\t" \
  "v_mfma_f32_16x16x32_bf16 v[20:23], v[220:223], v[236:239], 0\n\t" \
  "v_mfma_f32_16x16x32_bf16 v[24:27], v[224:227], v[236:239], 0\n\t" \
  "ds_read_b64_tr_b16 v[210:211], v247 offset:1024\n\t" \
  "v_mfma_f32_16x16x32_bf16 v[28:31], v[228:231], v[236:239], 0\n\t" \
  "s_waitcnt lgkmcnt(8)\n\t" \
  "v_mfma_f32_16x16x32_bf16 v[0:3], v[160:163], v[176:179], v[0:3]\n\t" \
  "ds_read_b64_tr_b16 v[232:233], v246 offset:8192\n\t" \
  "v_mfma_f32_16x16x32_bf16 v[4:7], v[164:167], v[176:179], v[4:7]\n\t" \
  "ds_read_b64_tr_b16 v[234:235], v246 offset:9216\n\t" \
  "v_mfma_f32_16x16x32_bf16 v[8:11], v[168:171], v[176:179], v[8:11]\n\t" \
  "ds_read_b128 v[216:219], v245 offset:0\n\t" \
  "v_mfma_f32_16x16x32_bf16 v[12:15], v[172:175], v[176:179], v[12:15]\n\t" \
  "ds_read_b128 v[220:223], v245 offset:2048\n\t" \
  "v_mfma_f32_16x16x32_bf16 v[16:19], v[160:163], v[180:183], v[16:19]\n\t" \
  "ds_read_b128 v[224:227], v245 offset:4096\n\t" \
  "v_mfma_f32_16x16x32_bf16 v[20:23], v[164:167], v[180:183], v[20:23]\n\t" \
  "ds_read_b128 v[228:231], v245 offset:6144\n\t" \
  "v_mfma_f32_16x16x32_bf16 v[24:27], v[168:171], v[180:183], v[24:27]\n\t" \
  "ds_read_b64_tr_b16 v[236:237], v247 offset:8192\n\t" \
  "v_mfma_f32_16x16x32_bf16 v[28:31], v[172:175], v[180:183], v[28:31]\n\t" \
  "ds_read_b64_tr_b16 v[238:239], v247 offset:9216\n\t" \
  "v_add_u32 v244, 0x8000, v244\n\t" \
  "v_add_u32 v245, 0x8000, v245\n\t" \
  "v_add_u32 v246, 0x8000, v246\n\t" \
  "v_add_u32 v247, 0x8000, v247\n\t" \
  "s_waitcnt lgkmcnt(8)\n\t" \
  "s_barrier\n\t" \
  "v_mfma_f32_16x16x32_bf16 v[0:3], v[188:191], v[204:207], v[0:3]\n\t" \
  "ds_read_b64_tr_b16 v[176:177], v246 offset:0\n\t" \
  "s_bfe_u32 %[t0], %[or2], 0x30014\n\t" \
  "v_mfma_f32_16x16x32_bf16 v[4:7], v[192:195], v[204:207], v[4:7]\n\t" \
  "ds_read_b64_tr_b16 v[178:179], v246 offset:1024\n\t" \
  "v_mfma_f32_16x16x32_bf16 v[8:11], v[196:199], v[204:207], v[8:11]\n\t" \
  "ds_read_b128 v[160:163], v244 offset:0\n\t" \
  "v_mfma_f32_16x16x32_bf16 v[12:15], v[200:203], v[204:207], v[12:15]\n\t" \
  "ds_read_b128 v[164:167], v244 offset:2048\n\t" \
  "v_mfma_f32_16x16x32_bf16 v[16:19], v[188:191], v[208:211], v[16:19]\n\t" \
  "ds_read_b128 v[168:171], v244 offset:4096\n\t" \
  "v_lshl_add_u32 v248, %[t0], 12, v253\n\t" \
  "v_mfma_f32_16x16x32_bf16 v[20:23], v[192:195], v[208:211], v[20:23]\n\t" \
  "ds_read_b128 v[172:175], v244 offset:6144\n\t" \
  "v_mfma_f32_16x16x32_bf16 v[24:27], v[196:199], v[208:211], v[24:27]\n\t" \
  "ds_read_b64_tr_b16 v[180:181], v247 offset:0\n\t" \
  "v_mfma_f32_16x16x32_bf16 v[28:31], v[200:203], v[208:211], v[28:31]\n\t" \
  "ds_read_b64_tr_b16 v[182:183], v247 offset:1024\n\t" \
  "s_waitcnt lgkmcnt(8)\n\t" \
  "v_mfma_f32_16x16x32_bf16 v[0:3], v[216:219], v[232:235], v[0:3]\n\t" \
  "ds_read_b64_tr_b16 v[204:205], v246 offset:8192\n\t" \
  "ds_read_b64_tr_b16 v[206:207], v246 offset:9216\n\t" \
  "v_mfma_f32_16x16x32_bf16 v[4:7], v[220:223], v[232:235], v[4:7]\n\t" \
  "ds_read_b128 v[188:191], v245 offset:0\n\t" \
  "v_mfma_f32_16x16x32_bf16 v[8:11], v[224:227], v[232:235], v[8:11]\n\t" \
  "ds_read_b128 v[192:195], v245 offset:2048\n\t" \
  "ds_read_b128 v[80:83], v248 offset:0\n\t" \
  "v_mfma_f32_16x16x32_bf16 v[12:15], v[228:231], v[232:235], v[12:15]\n\t" \
  "ds_read_b128 v[84:87], v248 offset:1024\n\t" \
  "v_mfma_f32_16x16x32_bf16 v[16:19], v[216:219], v[236:239], v[16:19]\n\t" \
  "ds_read_b128 v[88:91], v248 offset:2048\n\t" \
  "ds_read_b128 v[92:95], v248 offset:3072\n\t" \
  "v_cvt_pk_bf16_f32 v64, v0, v1\n\t" \
  "v_cvt_pk_bf16_f32 v65, v2, v3\n\t" \
  "v_mfma_f32_16x16x32_bf16 v[20:23], v[220:223], v[236:239], v[20:23]\n\t" \
  "ds_read_b128 v[196:199], v245 offset:4096\n\t" \
  "v_cvt_pk_bf16_f32 v66, v4, v5\n\t" \
  "v_cvt_pk_bf16_f32 v67, v6, v7\n\t" \
  "v_mfma_f32_16x16x32_bf16 v[24:27], v[224:227], v[236:239], v[24:27]\n\t" \
  "ds_read_b128 v[200:203], v245 offset:6144\n\t" \
  "ds_read_b64_tr_b16 v[208:209], v247 offset:8192\n\t" \
  "v_cvt_pk_bf16_f32 v68, v8, v9\n\t" \
  "v_cvt_pk_bf16_f32 v69, v10, v11\n\t" \
  "v_mfma_f32_16x16x32_bf16 v[28:31], v[228:231], v[236:239], v[28:31]\n\t" \
  "ds_read_b64_tr_b16 v[210:211], v247 offset:9216\n\t" \
  "v_cvt_pk_bf16_f32 v70, v12, v13\n\t" \
  "v_cvt_pk_bf16_f32 v71, v14, v15\n\t" \
  "v_add_u32 v244, 0xfffe8000, v244\n\t" \
  "v_add_u32 v245, 0xfffe8000, v245\n\t" \
  "v_add_u32 v246, 0xfffe8000, v246\n\t" \
  "v_add_u32 v247, 0xfffe8000, v247\n\t" \
  "s_waitcnt lgkmcnt(4)\n\t" \
  "s_barrier\n\t" \
  "v_mfma_f32_16x16x32_bf16 v[128:131], v[64:67], v[80:83], v[128:131]\n\t" \
  "ds_read_b64_tr_b16 v[232:233], v246 offset:0\n\t" \
  "v_cvt_pk_bf16_f32 v72, v16, v17\n\t" \
  "v_cvt_pk_bf16_f32 v73, v18, v19\n\t" \
  "v_mfma_f32_16x16x32_bf16 v[136:139], v[64:67], v[84:87], v[136:139]\n\t" \
  "v_cvt_pk_bf16_f32 v74, v20, v21\n\t" \
  "v_cvt_pk_bf16_f32 v75, v22, v23\n\t" \
  "v_mfma_f32_16x16x32_bf16 v[144:147], v[68:71], v[88:91], v[144:147]\n\t" \
  "ds_read_b64_tr_b16 v[234:235], v246 offset:1024\n\t" \
  "v_cvt_pk_bf16_f32 v76, v24, v25\n\t" \
  "v_cvt_pk_bf16_f32 v77, v26, v27\n\t" \
  "v_mfma_f32_16x16x32_bf16 v[152:155], v[68:71], v[92:95], v[152:155]\n\t" \
  "v_cvt_pk_bf16_f32 v78, v28, v29\n\t" \
  "v_cvt_pk_bf16_f32 v79, v30, v31\n\t" \
  "v_mfma_f32_16x16x32_bf16 v[0:3], v[160:163], v[176:179], 0\n\t" \
  "ds_read_b128 v[216:219], v244 offset:0\n\t" \
  "v_mfma_f32_16x16x32_bf16 v[4:7], v[164:167], v[176:179], 0\n\t" \
  "v_mfma_f32_16x16x32_bf16 v[8:11], v[168:171], v[176:179], 0\n\t" \
  "ds_read_b128 v[220:223], v244 offset:2048\n\t" \
  "v_mfma_f32_16x16x32_bf16 v[12:15], v[172:175], v[176:179], 0\n\t" \
  "v_mfma_f32_16x16x32_bf16 v[132:135], v[72:75], v[80:83], v[132:135]\n\t" \
  "ds_read_b128 v[224:227], v244 offset:4096\n\t" \
  "v_mfma_f32_16x16x32_bf16 v[140:143], v[72:75], v[84:87], v[140:143]\n\t" \
  "v_mfma_f32_16x16x32_bf16 v[148:151], v[76:79], v[88:91], v[148:151]\n\t" \
  "ds_read_b128 v[228:231], v244 offset:6144\n\t" \
  "v_mfma_f32_16x16x32_bf16 v[156:159], v[76:79], v[92:95], v[156:159]\n\t" \
  "v_mfma_f32_16x16x32_bf16 v[16:19], v[160:163], v[180:183], 0\n\t" \
  "ds_read_b64_tr_b16 v[236:237], v247 offset:0\n\t" \
  "v_mfma_f32_16x16x32_bf16 v[20:23], v[164:167], v[180:183], 0\n\t" \
  "v_mfma_f32_16x16x32_bf16 v[24:27], v[168:171], v[180:183], 0\n\t" \
  "ds_read_b64_tr_b16 v[238:239], v247 offset:1024\n\t" \
  "v_mfma_f32_16x16x32_bf16 v[28:31], v[172:175], v[180:183], 0\n\t" \
  "s_waitcnt lgkmcnt(8)\n\t" \
  "v_mfma_f32_16x16x32_bf16 v[0:3], v[188:191], v[204:207], v[0:3]\n\t" \
  "ds_read_b64_tr_b16 v[176:177], v246 offset:8192\n\t" \
  "v_mfma_f32_16x16x32_bf16 v[4:7], v[192:195], v[204:207], v[4:7]\n\t" \
  "ds_read_b64_tr_b16 v[178:179], v246 offset:9216\n\t" \
  "v_mfma_f32_16x16x32_bf16 v[8:11], v[196:199], v[204:207], v[8:11]\n\t" \
  "ds_read_b128 v[160:163], v245 offset:0\n\t" \
  "v_mfma_f32_16x16x32_bf16 v[12:15], v[200:203], v[204:207], v[12:15]\n\t" \
  "ds_read_b128 v[164:167], v245 offset:2048\n\t" \
  "v_mfma_f32_16x16x32_bf16 v[16:19], v[188:191], v[208:211], v[16:19]\n\t" \
  "ds_read_b128 v[168:171], v245 offset:4096\n\t" \
  "v_mfma_f32_16x16x32_bf16 v[20:23], v[192:195], v[208:211], v[20:23]\n\t" \
  "ds_read_b128 v[172:175], v245 offset:6144\n\t" \
  "v_mfma_f32_16x16x32_bf16 v[24:27], v[196:199], v[208:211], v[24:27]\n\t" \
  "ds_read_b64_tr_b16 v[180:181], v247 offset:8192\n\t" \
  "v_mfma_f32_16x16x32_bf16 v[28:31], v[200:203], v[208:211], v[28:31]\n\t" \
  "ds_read_b64_tr_b16 v[182:183], v247 offset:9216\n\t" \
  "v_add_u32 v244, 0x6000, v244\n\t" \
  "v_add_u32 v245, 0x6000, v245\n\t" \
  "v_add_u32 v246, 0xc000, v246\n\t" \
  "v_add_u32 v247, 0xc000, v247\n\t" \
  "s_waitcnt lgkmcnt(8)\n\t" \
  "s_barrier\n\t" \
  "v_mfma_f32_16x16x32_bf16 v[0:3], v[216:219], v[232:235], v[0:3]\n\t" \
  "ds_read_b64_tr_b16 v[204:205], v246 offset:0\n\t" \
  "s_bfe_u32 %[t0], %[or2], 0x30018\n\t" \
  "v_mfma_f32_16x16x32_bf16 v[4:7], v[220:223], v[232:235], v[4:7]\n\t" \
  "ds_read_b64_tr_b16 v[206:207], v246 offset:1024\n\t" \
  "v_mfma_f32_16x16x32_bf16 v[8:11], v[224:227], v[232:235], v[8:11]\n\t" \
  "ds_read_b128 v[188:191], v244 offset:0\n\t" \
  "v_mfma_f32_16x16x32_bf16 v[12:15], v[228:231], v[232:235], v[12:15]\n\t" \
  "ds_read_b128 v[192:195], v244 offset:2048\n\t" \
  "v_mfma_f32_16x16x32_bf16 v[16:19], v[216:219], v[236:239], v[16:19]\n\t" \
  "ds_read_b128 v[196:199], v244 offset:4096\n\t" \
  "v_lshl_add_u32 v248, %[t0], 12, v253\n\t" \
  "v_mfma_f32_16x16x32_bf16 v[20:23], v[220:223], v[236:239], v[20:23]\n\t" \
  "ds_read_b128 v[200:203], v244 offset:6144\n\t" \
  "v_mfma_f32_16x16x32_bf16 v[24:27], v[224:227], v[236:239], v[24:27]\n\t" \
  "ds_read_b64_tr_b16 v[208:209], v247 offset:0\n\t" \
  "v_mfma_f32_16x16x32_bf16 v[28:31], v[228:231], v[236:239], v[28:31]\n\t" \
  "ds_read_b64_tr_b16 v[210:211], v247 offset:1024\n\t" \
  "s_waitcnt lgkmcnt(8)\n\t" \
  "v_mfma_f32_16x16x32_bf16 v[0:3], v[160:163], v[176:179], v[0:3]\n\t" \
  "ds_read_b64_tr_b16 v[232:233], v246 offset:32768\n\t" \
  "ds_read_b64_tr_b16 v[234:235], v246 offset:33792\n\t" \
  "v_mfma_f32_16x16x32_bf16 v[4:7], v[164:167], v[176:179], v[4:7]\n\t" \
  "ds_read_b128 v[216:219], v245 offset:0\n\t" \
  "v_mfma_f32_16x16x32_bf16 v[8:11], v[168:171], v[176:179], v[8:11]\n\t" \
  "ds_read_b128 v[220:223], v245 offset:2048\n\t" \
  "ds_read_b128 v[80:83], v248 offset:0\n\t" \
  "v_mfma_f32_16x16x32_bf16 v[12:15], v[172:175], v[176:179], v[12:15]\n\t" \
  "ds_read_b128 v[84:87], v248 offset:1024\n\t" \
  "v_mfma_f32_16x16x32_bf16 v[16:19], v[160:163], v[180:183], v[16:19]\n\t" \
  "ds_read_b128 v[88:91], v248 offset:2048\n\t" \
  "ds_read_b128 v[92:95], v248 offset:3072\n\t" \
  "v_cvt_pk_bf16_f32 v64, v0, v1\n\t" \
  "v_cvt_pk_bf16_f32 v65, v2, v3\n\t" \
  "v_mfma_f32_16x16x32_bf16 v[20:23], v[164:167], v[180:183], v[20:23]\n\t" \
  "ds_read_b128 v[224:227], v245 offset:4096\n\t" \
  "v_cvt_pk_bf16_f32 v66, v4, v5\n\t" \
  "v_cvt_pk_bf16_f32 v67, v6, v7\n\t" \
  "v_mfma_f32_16x16x32_bf16 v[24:27], v[168:171], v[180:183], v[24:27]\n\t" \
  "ds_read_b128 v[228:231], v245 offset:6144\n\t" \
  "ds_read_b64_tr_b16 v[236:237], v247 offset:32768\n\t" \
  "v_cvt_pk_bf16_f32 v68, v8, v9\n\t" \
  "v_cvt_pk_bf16_f32 v69, v10, v11\n\t" \
  "v_mfma_f32_16x16x32_bf16 v[28:31], v[172:175], v[180:183], v[28:31]\n\t" \
  "ds_read_b64_tr_b16 v[238:239], v247 offset:33792\n\t" \
  "v_cvt_pk_bf16_f32 v70, v12, v13\n\t" \
  "v_cvt_pk_bf16_f32 v71, v14, v15\n\t" \
  "v_add_u32 v244, 0x2000, v244\n\t" \
  "v_add_u32 v245, 0x2000, v245\n\t" \
  "v_add_u32 v246, 0xffffc000, v246\n\t" \
  "v_add_u32 v247, 0xffffc000, v247\n\t" \
  "s_waitcnt lgkmcnt(4)\n\t" \
  "s_barrier\n\t" \
  "v_mfma_f32_16x16x32_bf16 v[128:131], v[64:67], v[80:83], v[128:131]\n\t" \
  "ds_read_b64_tr_b16 v[176:177], v246 offset:0\n\t" \
  "v_cvt_pk_bf16_f32 v72, v16, v17\n\t" \
  "v_cvt_pk_bf16_f32 v73, v18, v19\n\t" \
  "v_mfma_f32_16x16x32_bf16 v[136:139], v[64:67], v[84:87], v[136:139]\n\t" \
  "v_cvt_pk_bf16_f32 v74, v20, v21\n\t" \
  "v_cvt_pk_bf16_f32 v75, v22, v23\n\t" \
  "v_mfma_f32_16x16x32_bf16 v[144:147], v[68:71], v[88:91], v[144:147]\n\t" \
  "ds_read_b64_tr_b16 v[178:179], v246 offset:1024\n\t" \
  "v_cvt_pk_bf16_f32 v76, v24, v25\n\t" \
  "v_cvt_pk_bf16_f32 v77, v26, v27\n\t" \
  "v_mfma_f32_16x16x32_bf16 v[152:155], v[68:71], v[92:95], v[152:155]\n\t" \
  "v_cvt_pk_bf16_f32 v78, v28, v29\n\t" \
  "v_cvt_pk_bf16_f32 v79, v30, v31\n\t" \
  "v_mfma_f32_16x16x32_bf16 v[0:3], v[188:191], v[204:207], 0\n\t" \
  "ds_read_b128 v[160:163], v244 offset:0\n\t" \
  "v_mfma_f32_16x16x32_bf16 v[4:7], v[192:195], v[204:207], 0\n\t" \
  "v_mfma_f32_16x16x32_bf16 v[8:11], v[196:199], v[204:207], 0\n\t" \
  "ds_read_b128 v[164:167], v244 offset:2048\n\t" \
  "v_mfma_f32_16x16x32_bf16 v[12:15], v[200:203], v[204:207], 0\n\t" \
  "v_mfma_f32_16x16x32_bf16 v[132:135], v[72:75], v[80:83], v[132:135]\n\t" \
  "ds_read_b128 v[168:171], v244 offset:4096\n\t" \
  "v_mfma_f32_16x16x32_bf16 v[140:143], v[72:75], v[84:87], v[140:143]\n\t" \
  "v_mfma_f32_16x16x32_bf16 v[148:151], v[76:79], v[88:91], v[148:151]\n\t" \
  "ds_read_b128 v[172:175], v244 offset:6144\n\t" \
  "v_mfma_f32_16x16x32_bf16 v[156:159], v[76:79], v[92:95], v[156:159]\n\t" \
  "v_mfma_f32_16x16x32_bf16 v[16:19], v[188:191], v[208:211], 0\n\t" \
  "ds_read_b64_tr_b16 v[180:181], v247 offset:0\n\t" \
  "v_mfma_f32_16x16x32_bf16 v[20:23], v[192:195], v[208:211], 0\n\t" \
  "v_mfma_f32_16x16x32_bf16 v[24:27], v[196:199], v[208:211], 0\n\t" \
  "ds_read_b64_tr_b16 v[182:183], v247 offset:1024\n\t" \
  "v_mfma_f32_16x16x32_bf16 v[28:31], v[200:203], v[208:211], 0\n\t" \
  "s_waitcnt lgkmcnt(8)\n\t" \
  "v_mfma_f32_16x16x32_bf16 v[0:3], v[216:219], v[232:235], v[0:3]\n\t" \
  "ds_read_b64_tr_b16 v[204:205], v246 offset:8192\n\t" \
  "v_mfma_f32_16x16x32_bf16 v[4:7], v[220:223], v[232:235], v[4:7]\n\t" \
  "ds_read_b64_tr_b16 v[206:207], v246 offset:9216\n\t" \
  "v_mfma_f32_16x16x32_bf16 v[8:11], v[224:227], v[232:235], v[8:11]\n\t" \
  "ds_read_b128 v[188:191], v245 offset:0\n\t" \
  "v_mfma_f32_16x16x32_bf16 v[12:15], v[228:231], v[232:235], v[12:15]\n\t" \
  "ds_read_b128 v[192:195], v245 offset:2048\n\t" \
  "v_mfma_f32_16x16x32_bf16 v[16:19], v[216:219], v[236:239], v[16:19]\n\t" \
  "ds_read_b128 v[196:199], v245 offset:4096\n\t" \
  "v_mfma_f32_16x16x32_bf16 v[20:23], v[220:223], v[236:239], v[20:23]\n\t" \
  "ds_read_b128 v[200:203], v245 offset:6144\n\t" \
  "v_mfma_f32_16x16x32_bf16 v[24:27], v[224:227], v[236:239], v[24:27]\n\t" \
  "ds_read_b64_tr_b16 v[208:209], v247 offset:8192\n\t" \
  "v_mfma_f32_16x16x32_bf16 v[28:31], v[228:231], v[236:239], v[28:31]\n\t" \
  "ds_read_b64_tr_b16 v[210:211], v247 offset:9216\n\t" \
  "s_waitcnt lgkmcnt(8)\n\t" \
  "v_mfma_f32_16x16x32_bf16 v[0:3], v[160:163], v[176:179], v[0:3]\n\t" \
  "s_bfe_u32 %[t0], %[or2], 0x3001c\n\t" \
  "v_mfma_f32_16x16x32_bf16 v[4:7], v[164:167], v[176:179], v[4:7]\n\t" \
  "v_mfma_f32_16x16x32_bf16 v[8:11], v[168:171], v[176:179], v[8:11]\n\t" \
  "v_mfma_f32_16x16x32_bf16 v[12:15], v[172:175], v[176:179], v[12:15]\n\t" \
  "v_mfma_f32_16x16x32_bf16 v[16:19], v[160:163], v[180:183], v[16:19]\n\t" \
  "v_lshl_add_u32 v248, %[t0], 12, v253\n\t" \
  "v_mfma_f32_16x16x32_bf16 v[20:23], v[164:167], v[180:183], v[20:23]\n\t" \
  "v_mfma_f32_16x16x32_bf16 v[24:27], v[168:171], v[180:183], v[24:27]\n\t" \
  "v_mfma_f32_16x16x32_bf16 v[28:31], v[172:175], v[180:183], v[28:31]\n\t" \
  "s_waitcnt lgkmcnt(0)\n\t" \
  "v_mfma_f32_16x16x32_bf16 v[0:3], v[188:191], v[204:207], v[0:3]\n\t" \
  "ds_read_b128 v[80:83], v248 offset:0\n\t" \
  "v_mfma_f32_16x16x32_bf16 v[4:7], v[192:195], v[204:207], v[4:7]\n\t" \
  "v_mfma_f32_16x16x32_bf16 v[8:11], v[196:199], v[204:207], v[8:11]\n\t" \
  "ds_read_b128 v[84:87], v248 offset:1024\n\t" \
  "v_mfma_f32_16x16x32_bf16 v[12:15], v[200:203], v[204:207], v[12:15]\n\t" \
  "v_mfma_f32_16x16x32_bf16 v[16:19], v[188:191], v[208:211], v[16:19]\n\t" \
  "ds_read_b128 v[88:91], v248 offset:2048\n\t" \
  "s_nop 0\n\t" \
  "v_cvt_pk_bf16_f32 v64, v0, v1\n\t" \
  "v_cvt_pk_bf16_f32 v65, v2, v3\n\t" \
  "v_mfma_f32_16x16x32_bf16 v[20:23], v[192:195], v[208:211], v[20:23]\n\t" \
  "v_cvt_pk_bf16_f32 v66, v4, v5\n\t" \
  "v_cvt_pk_bf16_f32 v67, v6, v7\n\t" \
  "v_mfma_f32_16x16x32_bf16 v[24:27], v[196:199], v[208:211], v[24:27]\n\t" \
  "ds_read_b128 v[92:95], v248 offset:3072\n\t" \
  "v_cvt_pk_bf16_f32 v68, v8, v9\n\t" \
  "v_cvt_pk_bf16_f32 v69, v10, v11\n\t" \
  "v_mfma_f32_16x16x32_bf16 v[28:31], v[200:203], v[208:211], v[28:31]\n\t" \
  "v_cvt_pk_bf16_f32 v70, v12, v13\n\t" \
  "v_cvt_pk_bf16_f32 v71, v14, v15\n\t" \
  "s_waitcnt lgkmcnt(0)\n\t" \
  "s_nop 0\n\t" \
  "v_mfma_f32_16x16x32_bf16 v[152:155], v[68:71], v[92:95], v[152:155]\n\t" \
  "v_cvt_pk_bf16_f32 v76, v24, v25\n\t" \
  "v_cvt_pk_bf16_f32 v77, v26, v27\n\t" \
  "v_add_u32 v57, 0xfffe0000, v253\n\t" \
  "v_add_u32 v55, %[lownb], v57\n\t" \
  "v_mfma_f32_16x16x32_bf16 v[144:147], v[68:71], v[88:91], v[144:147]\n\t" \
  "v_cvt_pk_bf16_f32 v78, v28, v29\n\t" \
  "v_cvt_pk_bf16_f32 v79, v30, v31\n\t" \
  "s_xor_b32 %[t0], %[ks], 3\n\t" \
  "s_mul_i32 %[t0], %[t0], 24576\n\t" \
  "s_add_u32 %[t0], %[t0], %[sm]\n\t" \
  "s_add_u32 %[t0], %[t0], 8192\n\t" \
  "v_mfma_f32_16x16x32_bf16 v[136:139], v[64:67], v[84:87], v[136:139]\n\t" \
  "v_cvt_pk_bf16_f32 v72, v16, v17\n\t" \
  "v_cvt_pk_bf16_f32 v73, v18, v19\n\t" \
  "s_xor_b32 %[t1], %[ks], 2\n\t" \
  "s_mul_i32 %[t1], %[t1], 24576\n\t" \
  "s_add_u32 %[t1], %[t1], %[sm]\n\t" \
  "s_add_u32 %[t1], %[t1], 4096\n\t" \
  "v_mfma_f32_16x16x32_bf16 v[128:131], v[64:67], v[80:83], v[128:131]\n\t" \
  "v_cvt_pk_bf16_f32 v74, v20, v21\n\t" \
  "v_cvt_pk_bf16_f32 v75, v22, v23\n\t" \
  "s_xor_b32 %[t2], %[ks], 1\n\t" \
  "s_mul_i32 %[t2], %[t2], 24576\n\t" \
  "s_add_u32 %[t2], %[t2], %[sm]\n\t" \
  "v_mfma_f32_16x16x32_bf16 v[156:159], v[76:79], v[92:95], v[156:159]\n\t" \
  "v_mov_b32 v53, %[lown]\n\t" \
  "v_mov_b32 v54, 1\n\t" \
  "v_mfma_f32_16x16x32_bf16 v[148:151], v[76:79], v[88:91], v[148:151]\n\t" \
  "v_cvt_pk_bf16_f32 v152, v152, v153\n\t" \
  "v_cvt_pk_bf16_f32 v153, v154, v155\n\t" \
  "v_mfma_f32_16x16x32_bf16 v[140:143], v[72:75], v[84:87], v[140:143]\n\t" \
  "v_cvt_pk_bf16_f32 v144, v144, v145\n\t" \
  "v_cvt_pk_bf16_f32 v145, v146, v147\n\t" \
  "v_mfma_f32_16x16x32_bf16 v[132:135], v[72:75], v[80:83], v[132:135]\n\t" \
  "v_cvt_pk_bf16_f32 v136, v136, v137\n\t" \
  "v_cvt_pk_bf16_f32 v137, v138, v139\n\t" \
  "v_cvt_pk_bf16_f32 v154, v156, v157\n\t" \
  "v_cvt_pk_bf16_f32 v155, v158, v159\n\t" \
  "buffer_store_dwordx4 v[152:155], v57, %[rw], %[t0] offen offset:0 sc1\n\t" \
  "v_cvt_pk_bf16_f32 v146, v148, v149\n\t" \
  "v_cvt_pk_bf16_f32 v147, v150, v151\n\t" \
  "buffer_store_dwordx4 v[144:147], v57, %[rw], %[t1] offen offset:0 sc1\n\t" \
  "v_cvt_pk_bf16_f32 v138, v140, v141\n\t" \
  "v_cvt_pk_bf16_f32 v139, v142, v143\n\t" \
  "buffer_store_dwordx4 v[136:139], v57, %[rw], %[t2] offen offset:0 sc1\n\t" \
  "v_cvt_pk_bf16_f32 v128, v128, v129\n\t" \
  "v_cvt_pk_bf16_f32 v129, v130, v131\n\t" \
  "v_cvt_pk_bf16_f32 v130, v132, v133\n\t" \
  "v_cvt_pk_bf16_f32 v131, v134, v135\n\t" \
  "ds_write_b128 v55, v[128:131]\n\t" \
  "ds_write_b32 v53, v54\n\t" \
  "s_waitcnt vmcnt(0)\n\t" \
  "s_mov_b64 exec, 14\n\t" \
  "buffer_store_dwordx2 v[50:51], v52, %[rw], %[sfm] offen sc1\n\t" \
  "s_mov_b64 exec, -1\n\t"


#define LOAD_ASM_0 \
  "s_and_b32 s84, %[bid], 1\n\t" \
  "s_mul_i32 s84, s84, 40\n\t" \
  "s_add_u32 s86, s84, 24\n\t" \
  "s_load_dwordx2 s[76:77], %[ka], s84\n\t" \
  "s_load_dwordx2 s[88:89], %[ka], s86\n\t" \
  "s_getpc_b64 s[90:91]\n\t" \
  "v_lshrrev_b32 v251, 4, %[tid]\n\t" \
  "v_and_b32 v252, 3, %[tid]\n\t" \
  "v_and_or_b32 v251, v251, 28, v252\n\t" \
  "v_lshlrev_b32 v251, 7, v251\n\t" \
  "s_mov_b64 exec, 15\n\t" \
  "global_load_dword v251, v251, s[90:91]\n\t" \
  "s_mov_b64 exec, -1\n\t" \
  "s_mov_b32 %[z], 0\n\t"

#define LOAD_ASM_A \
  "s_cmp_lt_u32 %[wave], 4\n\t" \
  "s_cbranch_scc1 .Lsk1_%=\n\t" \
  "v_mbcnt_lo_u32_b32 v32, -1, 0\n\t" \
  "v_mbcnt_hi_u32_b32 v32, -1, v32\n\t" \
  "s_movk_i32 s82, 0x2000\n\t" \
  "s_mov_b32 m0, %[la]\n\t" \
  "v_lshrrev_b32 v33, 3, v32\n\t" \
  "v_lshrrev_b32 v34, 4, v32\n\t" \
  "v_and_b32 v35, 7, v32\n\t" \
  "v_xor_b32 v37, v35, v34\n\t" \
  "v_lshlrev_b32 v37, 4, v37\n\t" \
  "v_mul_u32_u24 v40, 1152, v33\n\t" \
  "v_add_u32 v0, v40, v37\n\t" \
  "v_xor_b32 v42, 64, v37\n\t" \
  "v_add3_u32 v1, v40, v42, s82\n\t" \
  "v_add_u32 v2, 16384, v0\n\t" \
  "v_add_u32 v3, 16384, v1\n\t" \
  "v_and_b32 v36, 15, v32\n\t" \
  "v_lshlrev_b32 v38, 4, v36\n\t" \
  "v_lshlrev_b32 v39, 5, v34\n\t" \
  "v_xor_b32 v38, v38, v39\n\t" \
  "v_lshl_add_u32 v4, v34, 13, v38\n\t" \
  "v_add_u32 v5, 31744, v4\n\t" \
  "v_xor_b32 v43, 0x80, v38\n\t" \
  "v_lshl_add_u32 v7, v34, 13, v43\n\t" \
  "v_add_u32 v6, 63488, v7\n\t" \
  "v_add_u32 v7, 95232, v7\n\t" \
  "s_mov_b32 s78, 589824\n\t" \
  "s_mov_b32 s79, 0x20000\n\t" \
  "s_mov_b32 s90, 4718592\n\t" \
  "s_mov_b32 s91, 0x20000\n\t" \
  "v_mov_b32 v20, 0\n\t" \
  "s_waitcnt lgkmcnt(0)\n\t" \
  "s_and_b32 s77, s77, 0xffff\n\t" \
  "s_and_b32 s89, s89, 0xffff\n\t" \
  "buffer_load_dwordx4 v0, s[76:79], %[sx] offen offset:0 lds\n\t" \
  "buffer_load_dwordx4 v1, s[76:79], %[sx] offen offset:1024 lds\n\t" \
  "buffer_load_dwordx4 v2, s[76:79], %[sx] offen offset:2048 lds\n\t" \
  "buffer_load_dwordx4 v3, s[76:79], %[sx] offen offset:3072 lds\n\t" \
  "s_add_u32 m0, %[la], 16384\n\t" \
  "s_nop 0\n\t" \
  "buffer_load_dwordx4 v4, s[88:91], %[sq] offen offset:0 lds\n\t" \
  "buffer_load_dwordx4 v5, s[88:91], %[sq] offen offset:1024 lds\n\t" \
  "buffer_load_dwordx4 v6, s[88:91], %[sq] offen offset:2048 lds\n\t" \
  "buffer_load_dwordx4 v7, s[88:91], %[sq] offen offset:3072 lds\n\t" \
  ".Lsk1_%=:\n\t" \
  "s_waitcnt lgkmcnt(0)\n\t" \
  "s_mov_b32 %[z], 0\n\t"

#define LOAD_ASM_B \
  "v_mov_b32 v20, 0\n\t" \
  "buffer_load_dwordx2 v[232:233], v20, %[rw], %[t0] offen sc1\n\t" \
  "v_mbcnt_lo_u32_b32 v32, -1, 0\n\t" \
  "v_mbcnt_hi_u32_b32 v32, -1, v32\n\t" \
  "s_movk_i32 s82, 0x2000\n\t" \
  "v_lshrrev_b32 v33, 3, v32\n\t" \
  "v_lshrrev_b32 v34, 4, v32\n\t" \
  "v_and_b32 v35, 7, v32\n\t" \
  "v_xor_b32 v37, v35, v34\n\t" \
  "v_lshlrev_b32 v37, 4, v37\n\t" \
  "v_mul_u32_u24 v40, 1152, v33\n\t" \
  "v_add_u32 v0, v40, v37\n\t" \
  "v_xor_b32 v42, 64, v37\n\t" \
  "v_add3_u32 v1, v40, v42, s82\n\t" \
  "v_add_u32 v2, 16384, v0\n\t" \
  "v_add_u32 v3, 16384, v1\n\t" \
  "v_and_b32 v36, 15, v32\n\t" \
  "v_lshlrev_b32 v38, 4, v36\n\t" \
  "v_lshlrev_b32 v39, 5, v34\n\t" \
  "v_xor_b32 v38, v38, v39\n\t" \
  "v_lshl_add_u32 v4, v34, 13, v38\n\t" \
  "v_add_u32 v5, 31744, v4\n\t" \
  "v_xor_b32 v43, 0x80, v38\n\t" \
  "v_lshl_add_u32 v7, v34, 13, v43\n\t" \
  "v_add_u32 v6, 63488, v7\n\t" \
  "v_add_u32 v7, 95232, v7\n\t" \
  "v_mul_u32_u24 v41, 1152, v34\n\t" \
  "v_add_u32 v16, v41, v38\n\t" \
  "v_add_u32 v17, v41, v38\n\t" \
  "v_add_u32 v17, 3584, v17\n\t" \
  "v_add_u32 v18, v41, v43\n\t" \
  "v_add_u32 v18, 7168, v18\n\t" \
  "v_add_u32 v19, v41, v43\n\t" \
  "v_add_u32 v19, 10752, v19\n\t" \
  "v_xor_b32 v8, v36, v34\n\t" \
  "v_lshlrev_b32 v8, 4, v8\n\t" \
  "v_lshl_add_u32 v8, v34, 8, v8\n\t" \
  "v_xor_b32 v9, 64, v8\n\t" \
  "v_xor_b32 v10, 128, v8\n\t" \
  "v_xor_b32 v11, 192, v8\n\t" \
  "v_lshl_add_u32 v12, v33, 8, v37\n\t" \
  "v_lshl_add_u32 v13, v33, 8, v42\n\t" \
  "v_add_u32 v13, 1024, v13\n\t" \
  "v_and_b32 v44, 1, v34\n\t" \
  "v_lshlrev_b32 v44, 5, v44\n\t" \
  "v_lshlrev_b32 v45, 4, v35\n\t" \
  "v_xor_b32 v44, v44, v45\n\t" \
  "v_add_u32 v14, v40, v44\n\t" \
  "v_xor_b32 v44, 64, v44\n\t" \
  "v_add3_u32 v15, v40, v44, s82\n\t" \
  "v_lshlrev_b32 v30, 4, v32\n\t" \
  "s_and_b32 %[t0], %[bid], 2\n\t" \
  "s_lshl_b32 %[t0], %[t0], 9\n\t" \
  "v_add_u32 v21, %[t0], v30\n\t" \
  "v_subrev_u32 v22, %[t0], v30\n\t" \
  "s_and_b32 %[t0], %[bid], 4\n\t" \
  "s_lshl_b32 %[t0], %[t0], 14\n\t" \
  "s_add_u32 s80, %[sp], %[t0]\n\t" \
  "s_sub_u32 s81, %[sp], %[t0]\n\t" \
  "s_add_u32 s81, s81, 65536\n\t" \
  "v_and_b32 v29, 3, v32\n\t" \
  "v_lshlrev_b32 v29, 3, v29\n\t" \
  "s_lshl_b32 s83, %[la], 1\n\t" \
  "s_add_u32 s83, s83, 131072\n\t" \
  "s_lshr_b32 s84, %[la], 1\n\t" \
  "s_lshr_b32 s86, %[la], 12\n\t" \
  "s_cmp_ge_u32 %[la], 8192\n\t" \
  "s_cselect_b32 s85, 24576, 0\n\t" \
  "s_add_u32 s85, s85, %[la]\n\t" \
  "s_add_u32 s85, s85, 57344\n\t" \
  "s_mov_b32 s72, 0\n\t" \
  "s_mov_b32 s73, 0\n\t" \
  "s_mov_b32 s74, 0\n\t" \
  "s_mov_b32 s75, 0\n\t" \
  "s_mov_b32 s76, 0\n\t" \
  "s_mov_b32 s77, 0\n\t" \
  "s_mov_b32 s78, 0\n\t" \
  "s_mov_b32 s79, 0\n\t" \
  "s_mov_b32 s87, 0\n\t" \
  "s_mov_b32 s61, 1048576\n\t" \
  "s_mov_b32 s62, 0\n\t" \
  "s_getpc_b64 s[90:91]\n\t" \
  ".Lpc2_%=:\n\t" \
  "s_add_u32 s90, s90, .Lph3_%=-.Lpc2_%=\n\t" \
  "s_addc_u32 s91, s91, 0\n\t" \
  "s_mov_b32 s60, 8\n\t" \
  "s_mov_b32 %[t0], 0\n\t" \
  ".Lp14_%=:\n\t" \
  "s_cmp_lg_u32 s60, %[kp]\n\t" \
  "s_cbranch_scc1 .Lnk5_%=\n\t" \
  "s_mov_b32 m0, s83\n\t" \
  "s_add_u32 %[t1], %[sk], 4096\n\t" \
  "buffer_load_dwordx4 v8, %[rk], %[sk] offen offset:0 lds\n\t" \
  "buffer_load_dwordx4 v9, %[rk], %[sk] offen offset:1024 lds\n\t" \
  "buffer_load_dwordx4 v10, %[rk], %[sk] offen offset:2048 lds\n\t" \
  "buffer_load_dwordx4 v11, %[rk], %[sk] offen offset:3072 lds\n\t" \
  "s_add_u32 m0, s83, 4096\n\t" \
  "s_nop 0\n\t" \
  "buffer_load_dwordx4 v8, %[rk], %[t1] offen offset:0 lds\n\t" \
  "buffer_load_dwordx4 v9, %[rk], %[t1] offen offset:1024 lds\n\t" \
  "buffer_load_dwordx4 v10, %[rk], %[t1] offen offset:2048 lds\n\t" \
  "buffer_load_dwordx4 v11, %[rk], %[t1] offen offset:3072 lds\n\t" \
  ".Lnk5_%=:\n\t" \
  "s_add_u32 %[t0], %[t0], 0x8000\n\t" \
  "s_and_b32 %[t0], %[t0], 0x18000\n\t" \
  "s_add_u32 %[t1], %[t0], %[la]\n\t" \
  "s_and_b32 s59, %[ord], 15\n\t" \
  "s_lshr_b32 %[ord], %[ord], 4\n\t" \
  "s_lshl_b32 s58, s59, 7\n\t" \
  "s_add_u32 s58, s58, %[sx]\n\t" \
  "s_lshl_b32 s59, s59, 19\n\t" \
  "s_mov_b32 m0, %[t1]\n\t" \
  "s_add_u32 s59, s59, %[sq]\n\t" \
  "buffer_load_dwordx4 v0, %[rx], s58 offen offset:0 lds\n\t" \
  "buffer_load_dwordx4 v1, %[rx], s58 offen offset:1024 lds\n\t" \
  "buffer_load_dwordx4 v2, %[rx], s58 offen offset:2048 lds\n\t" \
  "buffer_load_dwordx4 v3, %[rx], s58 offen offset:3072 lds\n\t" \
  "s_add_u32 m0, %[t1], 16384\n\t" \
  "s_sub_u32 s60, s60, 1\n\t" \
  "buffer_load_dwordx4 v4, %[rq], s59 offen offset:0 lds\n\t" \
  "buffer_load_dwordx4 v5, %[rq], s59 offen offset:1024 lds\n\t" \
  "buffer_load_dwordx4 v6, %[rq], s59 offen offset:2048 lds\n\t" \
  "buffer_load_dwordx4 v7, %[rq], s59 offen offset:3072 lds\n\t" \
  "s_waitcnt vmcnt(8)\n\t" \
  "s_barrier\n\t" \
  "s_cmp_lg_u32 s60, 0\n\t" \
  "s_cbranch_scc1 .Lp14_%=\n\t" \
  "s_lshl_b32 %[sk], %[nt], 8\n\t" \
  "s_sub_u32 %[sk], 1024, %[sk]\n\t" \
  "s_bfe_u32 s59, %[or2], 0x30000\n\t" \
  "s_lshr_b32 s58, s59, %[sh]\n\t" \
  "s_lshl_b32 s58, s58, 13\n\t" \
  "s_add_u32 s58, s58, %[sv]\n\t" \
  "s_mul_i32 s59, s59, 147456\n\t" \
  "s_add_u32 s59, s59, %[so]\n\t" \
  "s_add_u32 m0, s84, 32768\n\t" \
  "s_nop 0\n\t" \
  "buffer_load_dwordx4 v12, %[rv], s58 offen offset:0 lds\n\t" \
  "buffer_load_dwordx4 v13, %[rv], s58 offen offset:1024 lds\n\t" \
  "s_add_u32 m0, %[la], 40960\n\t" \
  "s_nop 0\n\t" \
  "buffer_load_dwordx4 v16, %[ro], s59 offen offset:0 lds\n\t" \
  "buffer_load_dwordx4 v17, %[ro], s59 offen offset:1024 lds\n\t" \
  "buffer_load_dwordx4 v18, %[ro], s59 offen offset:2048 lds\n\t" \
  "buffer_load_dwordx4 v19, %[ro], s59 offen offset:3072 lds\n\t" \
  "s_add_u32 s68, s59, %[sk]\n\t" \
  "v_readfirstlane_b32 s88, v232\n\t" \
  "v_readfirstlane_b32 s89, v233\n\t" \
  "s_nop 0\n\t" \
  "s_mul_hi_u32 s93, s88, 0x7f4a7c15\n\t" \
  "s_mul_i32 %[t1], s88, 0x9e3779b9\n\t" \
  "s_add_u32 s93, s93, %[t1]\n\t" \
  "s_mul_i32 %[t1], s89, 0x7f4a7c15\n\t" \
  "s_add_u32 s93, s93, %[t1]\n\t" \
  "s_mul_i32 s92, s88, 0x7f4a7c15\n\t" \
  "s_add_u32 s92, s92, 0xd192ed03\n\t" \
  "s_addc_u32 s93, s93, 0xd1b54a32\n\t" \
  "s_waitcnt vmcnt(6)\n\t" \
  "s_barrier\n\t" \
  "s_xor_b32 s58, s58, 128\n\t" \
  "s_add_u32 s59, s59, %[dw]\n\t" \
  "s_add_u32 m0, s84, 65536\n\t" \
  "s_nop 0\n\t" \
  "buffer_load_dwordx4 v12, %[rv], s58 offen offset:0 lds\n\t" \
  "buffer_load_dwordx4 v13, %[rv], s58 offen offset:1024 lds\n\t" \
  "s_add_u32 m0, %[la], 73728\n\t" \
  "s_nop 0\n\t" \
  "buffer_load_dwordx4 v16, %[ro], s59 offen offset:0 lds\n\t" \
  "buffer_load_dwordx4 v17, %[ro], s59 offen offset:1024 lds\n\t" \
  "buffer_load_dwordx4 v18, %[ro], s59 offen offset:2048 lds\n\t" \
  "buffer_load_dwordx4 v19, %[ro], s59 offen offset:3072 lds\n\t" \
  "s_add_u32 s69, s59, %[sk]\n\t" \
  "s_barrier\n\t" \
  "s_bfe_u32 s59, %[or2], 0x30004\n\t" \
  "s_lshr_b32 s58, s59, %[sh]\n\t" \
  "s_lshl_b32 s58, s58, 13\n\t" \
  "s_add_u32 s58, s58, %[sv]\n\t" \
  "s_mul_i32 s59, s59, 147456\n\t" \
  "s_add_u32 s59, s59, %[so]\n\t" \
  "s_add_u32 m0, s84, 98304\n\t" \
  "s_nop 0\n\t" \
  "buffer_load_dwordx4 v12, %[rv], s58 offen offset:0 lds\n\t" \
  "buffer_load_dwordx4 v13, %[rv], s58 offen offset:1024 lds\n\t" \
  "s_add_u32 m0, %[la], 106496\n\t" \
  "s_nop 0\n\t" \
  "buffer_load_dwordx4 v16, %[ro], s59 offen offset:0 lds\n\t" \
  "buffer_load_dwordx4 v17, %[ro], s59 offen offset:1024 lds\n\t" \
  "buffer_load_dwordx4 v18, %[ro], s59 offen offset:2048 lds\n\t" \
  "buffer_load_dwordx4 v19, %[ro], s59 offen offset:3072 lds\n\t" \
  "s_add_u32 s70, s59, %[sk]\n\t" \
  "s_barrier\n\t" \
  "s_add_u32 m0, s84, 131072\n\t" \
  "s_nop 0\n\t" \
  "buffer_load_dwordx4 v14, %[ro], s68 offen offset:0 lds\n\t" \
  "buffer_load_dwordx4 v15, %[ro], s68 offen offset:1024 lds\n\t" \
  "s_add_u32 m0, s84, 139264\n\t" \
  "s_nop 0\n\t" \
  "buffer_load_dwordx4 v14, %[ro], s69 offen offset:0 lds\n\t" \
  "buffer_load_dwordx4 v15, %[ro], s69 offen offset:1024 lds\n\t" \
  "s_add_u32 m0, s84, 147456\n\t" \
  "s_nop 0\n\t" \
  "buffer_load_dwordx4 v14, %[ro], s70 offen offset:0 lds\n\t" \
  "buffer_load_dwordx4 v15, %[ro], s70 offen offset:1024 lds\n\t" \
  "s_xor_b32 s58, s58, 128\n\t" \
  "s_add_u32 s59, s59, %[dw]\n\t" \
  "s_add_u32 m0, s84, 0\n\t" \
  "s_nop 0\n\t" \
  "buffer_load_dwordx4 v12, %[rv], s58 offen offset:0 lds\n\t" \
  "buffer_load_dwordx4 v13, %[rv], s58 offen offset:1024 lds\n\t" \
  "s_add_u32 m0, %[la], 8192\n\t" \
  "s_nop 0\n\t" \
  "buffer_load_dwordx4 v16, %[ro], s59 offen offset:0 lds\n\t" \
  "buffer_load_dwordx4 v17, %[ro], s59 offen offset:1024 lds\n\t" \
  "buffer_load_dwordx4 v18, %[ro], s59 offen offset:2048 lds\n\t" \
  "buffer_load_dwordx4 v19, %[ro], s59 offen offset:3072 lds\n\t" \
  "s_add_u32 s71, s59, %[sk]\n\t" \
  "s_add_u32 m0, s84, 155648\n\t" \
  "s_nop 0\n\t" \
  "buffer_load_dwordx4 v14, %[ro], s71 offen offset:0 lds\n\t" \
  "buffer_load_dwordx4 v15, %[ro], s71 offen offset:1024 lds\n\t" \
  "s_waitcnt vmcnt(8)\n\t" \
  "s_barrier\n\t" \
  "s_bfe_u32 s59, %[or2], 0x30008\n\t" \
  "s_lshr_b32 s58, s59, %[sh]\n\t" \
  "s_lshl_b32 s58, s58, 13\n\t" \
  "s_add_u32 s58, s58, %[sv]\n\t" \
  "s_mul_i32 s59, s59, 147456\n\t" \
  "s_add_u32 s59, s59, %[so]\n\t" \
  "s_add_u32 m0, s84, 24576\n\t" \
  "s_nop 0\n\t" \
  "buffer_load_dwordx4 v12, %[rv], s58 offen offset:0 lds\n\t" \
  "buffer_load_dwordx4 v13, %[rv], s58 offen offset:1024 lds\n\t" \
  "s_mov_b32 m0, s85\n\t" \
  "s_nop 0\n\t" \
  "buffer_load_dwordx4 v16, %[ro], s59 offen offset:0 lds\n\t" \
  "buffer_load_dwordx4 v17, %[ro], s59 offen offset:1024 lds\n\t" \
  "buffer_load_dwordx4 v18, %[ro], s59 offen offset:2048 lds\n\t" \
  "buffer_load_dwordx4 v19, %[ro], s59 offen offset:3072 lds\n\t" \
  "s_barrier\n\t" \
  "s_xor_b32 s58, s58, 128\n\t" \
  "s_add_u32 s59, s59, %[dw]\n\t" \
  "s_add_u32 m0, s84, 32768\n\t" \
  "s_nop 0\n\t" \
  "buffer_load_dwordx4 v12, %[rv], s58 offen offset:0 lds\n\t" \
  "buffer_load_dwordx4 v13, %[rv], s58 offen offset:1024 lds\n\t" \
  "s_add_u32 m0, %[la], 40960\n\t" \
  "s_nop 0\n\t" \
  "buffer_load_dwordx4 v16, %[ro], s59 offen offset:0 lds\n\t" \
  "buffer_load_dwordx4 v17, %[ro], s59 offen offset:1024 lds\n\t" \
  "buffer_load_dwordx4 v18, %[ro], s59 offen offset:2048 lds\n\t" \
  "buffer_load_dwordx4 v19, %[ro], s59 offen offset:3072 lds\n\t" \
  "s_waitcnt vmcnt(12)\n\t" \
  "s_barrier\n\t" \
  "s_cmp_eq_u32 s62, -1\n\t" \
  "s_cbranch_scc1 .Lpc6_%=\n\t" \
  "s_swappc_b64 s[88:89], s[90:91]\n\t" \
  ".Lpc6_%=:\n\t" \
  "s_bfe_u32 s59, %[or2], 0x3000c\n\t" \
  "s_lshr_b32 s58, s59, %[sh]\n\t" \
  "s_lshl_b32 s58, s58, 13\n\t" \
  "s_add_u32 s58, s58, %[sv]\n\t" \
  "s_mul_i32 s59, s59, 147456\n\t" \
  "s_add_u32 s59, s59, %[so]\n\t" \
  "s_add_u32 m0, s84, 65536\n\t" \
  "s_nop 0\n\t" \
  "buffer_load_dwordx4 v12, %[rv], s58 offen offset:0 lds\n\t" \
  "buffer_load_dwordx4 v13, %[rv], s58 offen offset:1024 lds\n\t" \
  "s_add_u32 m0, %[la], 73728\n\t" \
  "s_nop 0\n\t" \
  "buffer_load_dwordx4 v16, %[ro], s59 offen offset:0 lds\n\t" \
  "buffer_load_dwordx4 v17, %[ro], s59 offen offset:1024 lds\n\t" \
  "buffer_load_dwordx4 v18, %[ro], s59 offen offset:2048 lds\n\t" \
  "buffer_load_dwordx4 v19, %[ro], s59 offen offset:3072 lds\n\t" \
  "s_waitcnt vmcnt(12)\n\t" \
  "s_barrier\n\t" \
  "s_cmp_eq_u32 s62, -1\n\t" \
  "s_cbranch_scc1 .Lpc7_%=\n\t" \
  "s_swappc_b64 s[88:89], s[90:91]\n\t" \
  ".Lpc7_%=:\n\t" \
  "s_xor_b32 s58, s58, 128\n\t" \
  "s_add_u32 s59, s59, %[dw]\n\t" \
  "s_add_u32 m0, s84, 98304\n\t" \
  "s_nop 0\n\t" \
  "buffer_load_dwordx4 v12, %[rv], s58 offen offset:0 lds\n\t" \
  "buffer_load_dwordx4 v13, %[rv], s58 offen offset:1024 lds\n\t" \
  "s_add_u32 m0, %[la], 106496\n\t" \
  "s_nop 0\n\t" \
  "buffer_load_dwordx4 v16, %[ro], s59 offen offset:0 lds\n\t" \
  "buffer_load_dwordx4 v17, %[ro], s59 offen offset:1024 lds\n\t" \
  "buffer_load_dwordx4 v18, %[ro], s59 offen offset:2048 lds\n\t" \
  "buffer_load_dwordx4 v19, %[ro], s59 offen offset:3072 lds\n\t" \
  "s_waitcnt vmcnt(12)\n\t" \
  "s_barrier\n\t" \
  "s_mov_b32 s87, 1\n\t" \
  "s_cmp_eq_u32 s62, -1\n\t" \
  "s_cbranch_scc1 .Lpc8_%=\n\t" \
  "s_swappc_b64 s[88:89], s[90:91]\n\t" \
  ".Lpc8_%=:\n\t" \
  "s_cmp_eq_u32 s62, -1\n\t" \
  "s_cbranch_scc1 .Lwa9_%=\n\t" \
  "s_waitcnt vmcnt(6)\n\t" \
  ".Lwa9_%=:\n\t" \
  "s_waitcnt vmcnt(14)\n\t" \
  "s_barrier\n\t" \
  "s_cmp_eq_u32 s62, -1\n\t" \
  "s_cbranch_scc1 .Lpc11_%=\n\t" \
  "s_swappc_b64 s[88:89], s[90:91]\n\t" \
  ".Lpc11_%=:\n\t" \
  "s_bfe_u32 s59, %[or2], 0x30010\n\t" \
  "s_lshr_b32 s58, s59, %[sh]\n\t" \
  "s_lshl_b32 s58, s58, 13\n\t" \
  "s_add_u32 s58, s58, %[sv]\n\t" \
  "s_mul_i32 s59, s59, 147456\n\t" \
  "s_add_u32 s59, s59, %[so]\n\t" \
  "s_add_u32 m0, s84, 0\n\t" \
  "s_nop 0\n\t" \
  "buffer_load_dwordx4 v12, %[rv], s58 offen offset:0 lds\n\t" \
  "buffer_load_dwordx4 v13, %[rv], s58 offen offset:1024 lds\n\t" \
  "s_add_u32 m0, %[la], 8192\n\t" \
  "s_nop 0\n\t" \
  "buffer_load_dwordx4 v16, %[ro], s59 offen offset:0 lds\n\t" \
  "buffer_load_dwordx4 v17, %[ro], s59 offen offset:1024 lds\n\t" \
  "buffer_load_dwordx4 v18, %[ro], s59 offen offset:2048 lds\n\t" \
  "buffer_load_dwordx4 v19, %[ro], s59 offen offset:3072 lds\n\t" \
  "s_cmp_eq_u32 s62, -1\n\t" \
  "s_cbranch_scc1 .Lwa12_%=\n\t" \
  "s_waitcnt vmcnt(6)\n\t" \
  ".Lwa12_%=:\n\t" \
  "s_waitcnt vmcnt(14)\n\t" \
  "s_barrier\n\t" \
  ".Lpf14_%=:\n\t" \
  "s_cmp_eq_u32 s62, -1\n\t" \
  "s_cbranch_scc1 .Lpd16_%=\n\t" \
  "s_swappc_b64 s[88:89], s[90:91]\n\t" \
  "s_waitcnt vmcnt(0)\n\t" \
  "s_sub_i32 s61, s61, 1\n\t" \
  "s_cmp_gt_i32 s61, 0\n\t" \
  "s_cbranch_scc1 .Lpf14_%=\n\t" \
  ".Lpd16_%=:\n\t" \
  "s_xor_b32 s58, s58, 128\n\t" \
  "s_add_u32 s59, s59, %[dw]\n\t" \
  "s_add_u32 m0, s84, 24576\n\t" \
  "s_nop 0\n\t" \
  "buffer_load_dwordx4 v12, %[rv], s58 offen offset:0 lds\n\t" \
  "buffer_load_dwordx4 v13, %[rv], s58 offen offset:1024 lds\n\t" \
  "s_mov_b32 m0, s85\n\t" \
  "s_nop 0\n\t" \
  "buffer_load_dwordx4 v16, %[ro], s59 offen offset:0 lds\n\t" \
  "buffer_load_dwordx4 v17, %[ro], s59 offen offset:1024 lds\n\t" \
  "buffer_load_dwordx4 v18, %[ro], s59 offen offset:2048 lds\n\t" \
  "buffer_load_dwordx4 v19, %[ro], s59 offen offset:3072 lds\n\t" \
  "s_waitcnt vmcnt(6)\n\t" \
  "s_barrier\n\t" \
  "s_bfe_u32 s59, %[or2], 0x30014\n\t" \
  "s_lshr_b32 s58, s59, %[sh]\n\t" \
  "s_lshl_b32 s58, s58, 13\n\t" \
  "s_add_u32 s58, s58, %[sv]\n\t" \
  "s_mul_i32 s59, s59, 147456\n\t" \
  "s_add_u32 s59, s59, %[so]\n\t" \
  "s_add_u32 m0, s84, 32768\n\t" \
  "s_nop 0\n\t" \
  "buffer_load_dwordx4 v12, %[rv], s58 offen offset:0 lds\n\t" \
  "buffer_load_dwordx4 v13, %[rv], s58 offen offset:1024 lds\n\t" \
  "s_add_u32 m0, %[la], 40960\n\t" \
  "s_nop 0\n\t" \
  "buffer_load_dwordx4 v16, %[ro], s59 offen offset:0 lds\n\t" \
  "buffer_load_dwordx4 v17, %[ro], s59 offen offset:1024 lds\n\t" \
  "buffer_load_dwordx4 v18, %[ro], s59 offen offset:2048 lds\n\t" \
  "buffer_load_dwordx4 v19, %[ro], s59 offen offset:3072 lds\n\t" \
  "s_xor_b32 s58, s58, 128\n\t" \
  "s_add_u32 s59, s59, %[dw]\n\t" \
  "s_add_u32 m0, s84, 65536\n\t" \
  "s_nop 0\n\t" \
  "buffer_load_dwordx4 v12, %[rv], s58 offen offset:0 lds\n\t" \
  "buffer_load_dwordx4 v13, %[rv], s58 offen offset:1024 lds\n\t" \
  "s_add_u32 m0, %[la], 73728\n\t" \
  "s_nop 0\n\t" \
  "buffer_load_dwordx4 v16, %[ro], s59 offen offset:0 lds\n\t" \
  "buffer_load_dwordx4 v17, %[ro], s59 offen offset:1024 lds\n\t" \
  "buffer_load_dwordx4 v18, %[ro], s59 offen offset:2048 lds\n\t" \
  "buffer_load_dwordx4 v19, %[ro], s59 offen offset:3072 lds\n\t" \
  "s_waitcnt vmcnt(12)\n\t" \
  "s_barrier\n\t" \
  "s_bfe_u32 s59, %[or2], 0x30018\n\t" \
  "s_lshr_b32 s58, s59, %[sh]\n\t" \
  "s_lshl_b32 s58, s58, 13\n\t" \
  "s_add_u32 s58, s58, %[sv]\n\t" \
  "s_mul_i32 s59, s59, 147456\n\t" \
  "s_add_u32 s59, s59, %[so]\n\t" \
  "s_add_u32 m0, s84, 98304\n\t" \
  "s_nop 0\n\t" \
  "buffer_load_dwordx4 v12, %[rv], s58 offen offset:0 lds\n\t" \
  "buffer_load_dwordx4 v13, %[rv], s58 offen offset:1024 lds\n\t" \
  "s_add_u32 m0, %[la], 106496\n\t" \
  "s_nop 0\n\t" \
  "buffer_load_dwordx4 v16, %[ro], s59 offen offset:0 lds\n\t" \
  "buffer_load_dwordx4 v17, %[ro], s59 offen offset:1024 lds\n\t" \
  "buffer_load_dwordx4 v18, %[ro], s59 offen offset:2048 lds\n\t" \
  "buffer_load_dwordx4 v19, %[ro], s59 offen offset:3072 lds\n\t" \
  "s_waitcnt vmcnt(12)\n\t" \
  "s_barrier\n\t" \
  "s_xor_b32 s58, s58, 128\n\t" \
  "s_add_u32 s59, s59, %[dw]\n\t" \
  "s_add_u32 m0, s84, 0\n\t" \
  "s_nop 0\n\t" \
  "buffer_load_dwordx4 v12, %[rv], s58 offen offset:0 lds\n\t" \
  "buffer_load_dwordx4 v13, %[rv], s58 offen offset:1024 lds\n\t" \
  "s_add_u32 m0, %[la], 8192\n\t" \
  "s_nop 0\n\t" \
  "buffer_load_dwordx4 v16, %[ro], s59 offen offset:0 lds\n\t" \
  "buffer_load_dwordx4 v17, %[ro], s59 offen offset:1024 lds\n\t" \
  "buffer_load_dwordx4 v18, %[ro], s59 offen offset:2048 lds\n\t" \
  "buffer_load_dwordx4 v19, %[ro], s59 offen offset:3072 lds\n\t" \
  "s_waitcnt vmcnt(12)\n\t" \
  "s_barrier\n\t" \
  "s_bfe_u32 s59, %[or2], 0x3001c\n\t" \
  "s_lshr_b32 s58, s59, %[sh]\n\t" \
  "s_lshl_b32 s58, s58, 13\n\t" \
  "s_add_u32 s58, s58, %[sv]\n\t" \
  "s_mul_i32 s59, s59, 147456\n\t" \
  "s_add_u32 s59, s59, %[so]\n\t" \
  "s_add_u32 m0, s84, 24576\n\t" \
  "s_nop 0\n\t" \
  "buffer_load_dwordx4 v12, %[rv], s58 offen offset:0 lds\n\t" \
  "buffer_load_dwordx4 v13, %[rv], s58 offen offset:1024 lds\n\t" \
  "s_mov_b32 m0, s85\n\t" \
  "s_nop 0\n\t" \
  "buffer_load_dwordx4 v16, %[ro], s59 offen offset:0 lds\n\t" \
  "buffer_load_dwordx4 v17, %[ro], s59 offen offset:1024 lds\n\t" \
  "buffer_load_dwordx4 v18, %[ro], s59 offen offset:2048 lds\n\t" \
  "buffer_load_dwordx4 v19, %[ro], s59 offen offset:3072 lds\n\t" \
  "s_waitcnt vmcnt(12)\n\t" \
  "s_barrier\n\t" \
  "s_xor_b32 s58, s58, 128\n\t" \
  "s_add_u32 s59, s59, %[dw]\n\t" \
  "s_add_u32 m0, s84, 32768\n\t" \
  "s_nop 0\n\t" \
  "buffer_load_dwordx4 v12, %[rv], s58 offen offset:0 lds\n\t" \
  "buffer_load_dwordx4 v13, %[rv], s58 offen offset:1024 lds\n\t" \
  "s_add_u32 m0, %[la], 40960\n\t" \
  "s_nop 0\n\t" \
  "buffer_load_dwordx4 v16, %[ro], s59 offen offset:0 lds\n\t" \
  "buffer_load_dwordx4 v17, %[ro], s59 offen offset:1024 lds\n\t" \
  "buffer_load_dwordx4 v18, %[ro], s59 offen offset:2048 lds\n\t" \
  "buffer_load_dwordx4 v19, %[ro], s59 offen offset:3072 lds\n\t" \
  "s_waitcnt lgkmcnt(0)\n\t" \
  "v_lshlrev_b32 v47, 3, v36\n\t" \
  "s_lshl_b32 s72, %[bid], 7\n\t" \
  "s_add_u32 s72, s72, 659456\n\t" \
  "buffer_load_dwordx2 v[232:233], v47, %[rw], s72 offen sc1\n\t" \
  "s_waitcnt vmcnt(13)\n\t" \
  "s_barrier\n\t" \
  "v_lshlrev_b32 v46, 9, v33\n\t" \
  "v_lshl_add_u32 v46, v35, 4, v46\n\t" \
  "s_lshl_b32 %[t1], s86, 7\n\t" \
  "v_add_u32 v46, %[t1], v46\n\t" \
  "s_lshl_b32 s73, %[bid], 13\n\t" \
  "s_add_u32 s73, s73, 5935104\n\t" \
  "s_add_u32 s58, s73, 4096\n\t" \
  "s_mul_i32 s74, %[bid], 12288\n\t" \
  "s_add_u32 s74, s74, 2789376\n\t" \
  "s_waitcnt vmcnt(7)\n\t" \
  "s_barrier\n\t" \
  "s_waitcnt vmcnt(1)\n\t" \
  "s_barrier\n\t" \
  ".Lfa17_%=:\n\t" \
  "s_waitcnt vmcnt(0)\n\t" \
  "v_cmp_ne_u64 vcc, s[92:93], v[232:233]\n\t" \
  "s_cbranch_vccz .Lfb18_%=\n\t" \
  "buffer_load_dwordx2 v[232:233], v47, %[rw], s72 offen sc1\n\t" \
  "s_sub_i32 s61, s61, 1\n\t" \
  "s_cmp_gt_i32 s61, 0\n\t" \
  "s_cbranch_scc1 .Lfa17_%=\n\t" \
  ".Lfb18_%=:\n\t" \
  "buffer_load_dwordx4 v[200:203], v46, %[rw], s73 offen sc1\n\t" \
  "buffer_load_dwordx4 v[204:207], v46, %[rw], s58 offen sc1\n\t" \
  "v_lshrrev_b32 v48, 3, v36\n\t" \
  "v_lshl_add_u32 v48, v35, 1, v48\n\t" \
  "v_mul_u32_u24 v48, 2304, v48\n\t" \
  "s_mul_i32 %[t1], %[nt], 224\n\t" \
  "s_sub_u32 %[t1], 1024, %[t1]\n\t" \
  "s_lshl_b32 s58, s86, 3\n\t" \
  "s_add_u32 %[t1], %[t1], s58\n\t" \
  "s_add_u32 %[t1], %[t1], %[oo]\n\t" \
  "v_mul_u32_u24 v49, 2304, v36\n\t" \
  "v_and_b32 v50, 1, v34\n\t" \
  "v_lshl_add_u32 v49, v50, 5, v49\n\t" \
  "v_lshrrev_b32 v50, 1, v34\n\t" \
  "v_lshl_add_u32 v49, v50, 4, v49\n\t" \
  "s_lshl_b32 s59, s86, 6\n\t" \
  "s_add_u32 s59, s59, %[oo]\n\t" \
  "s_lshl_b32 s58, s86, 10\n\t" \
  "s_add_u32 %[t0], s58, 122880\n\t" \
  "v_add_u32 v51, %[t0], v30\n\t" \
  "s_add_u32 s74, s74, s58\n\t" \
  "s_lshl_b32 %[t0], s86, 2\n\t" \
  "s_add_u32 %[t0], %[t0], 126976\n\t" \
  "v_mov_b32 v52, %[t0]\n\t" \
  "s_mov_b32 s64, 0xffff0000\n\t" \
  "s_add_u32 s65, s74, 4096\n\t" \
  "s_add_u32 s66, s74, 8192\n\t" \
  "s_waitcnt vmcnt(0)\n\t" \
  "v_lshlrev_b32 v208, 16, v200\n\t" \
  "v_and_b32 v209, s64, v200\n\t" \
  "v_lshlrev_b32 v216, 16, v204\n\t" \
  "v_and_b32 v217, s64, v204\n\t" \
  "v_lshlrev_b32 v210, 16, v201\n\t" \
  "v_and_b32 v211, s64, v201\n\t" \
  "v_lshlrev_b32 v218, 16, v205\n\t" \
  "v_and_b32 v219, s64, v205\n\t" \
  "v_lshlrev_b32 v212, 16, v202\n\t" \
  "v_and_b32 v213, s64, v202\n\t" \
  "v_lshlrev_b32 v220, 16, v206\n\t" \
  "v_and_b32 v221, s64, v206\n\t" \
  "v_lshlrev_b32 v214, 16, v203\n\t" \
  "v_and_b32 v215, s64, v203\n\t" \
  "v_lshlrev_b32 v222, 16, v207\n\t" \
  "v_and_b32 v223, s64, v207\n\t" \
  "v_add_f32 v208, v208, v216\n\t" \
  "v_add_f32 v209, v209, v217\n\t" \
  "v_add_f32 v210, v210, v218\n\t" \
  "v_add_f32 v211, v211, v219\n\t" \
  "v_add_f32 v212, v212, v220\n\t" \
  "v_add_f32 v213, v213, v221\n\t" \
  "v_add_f32 v214, v214, v222\n\t" \
  "v_add_f32 v215, v215, v223\n\t" \
  "v_add_f32_dpp v208, v208, v208 row_ror:8 row_mask:0xf bank_mask:0xf\n\t" \
  "v_add_f32_dpp v209, v209, v209 row_ror:8 row_mask:0xf bank_mask:0xf\n\t" \
  "v_add_f32_dpp v210, v210, v210 row_ror:8 row_mask:0xf bank_mask:0xf\n\t" \
  "v_add_f32_dpp v211, v211, v211 row_ror:8 row_mask:0xf bank_mask:0xf\n\t" \
  "v_add_f32_dpp v212, v212, v212 row_ror:8 row_mask:0xf bank_mask:0xf\n\t" \
  "v_add_f32_dpp v213, v213, v213 row_ror:8 row_mask:0xf bank_mask:0xf\n\t" \
  "v_add_f32_dpp v214, v214, v214 row_ror:8 row_mask:0xf bank_mask:0xf\n\t" \
  "v_add_f32_dpp v215, v215, v215 row_ror:8 row_mask:0xf bank_mask:0xf\n\t" \
  "v_mov_b32 v216, v208\n\t" \
  "v_mov_b32 v217, v209\n\t" \
  "v_mov_b32 v218, v210\n\t" \
  "v_mov_b32 v219, v211\n\t" \
  "v_mov_b32 v220, v212\n\t" \
  "v_mov_b32 v221, v213\n\t" \
  "v_mov_b32 v222, v214\n\t" \
  "v_mov_b32 v223, v215\n\t" \
  "v_permlane16_swap_b32 v208, v216\n\t" \
  "v_permlane16_swap_b32 v209, v217\n\t" \
  "v_permlane16_swap_b32 v210, v218\n\t" \
  "v_permlane16_swap_b32 v211, v219\n\t" \
  "v_permlane16_swap_b32 v212, v220\n\t" \
  "v_permlane16_swap_b32 v213, v221\n\t" \
  "v_permlane16_swap_b32 v214, v222\n\t" \
  "v_permlane16_swap_b32 v215, v223\n\t" \
  "v_add_f32 v208, v208, v216\n\t" \
  "v_add_f32 v209, v209, v217\n\t" \
  "v_add_f32 v210, v210, v218\n\t" \
  "v_add_f32 v211, v211, v219\n\t" \
  "v_add_f32 v212, v212, v220\n\t" \
  "v_add_f32 v213, v213, v221\n\t" \
  "v_add_f32 v214, v214, v222\n\t" \
  "v_add_f32 v215, v215, v223\n\t" \
  "v_mov_b32 v216, v208\n\t" \
  "v_mov_b32 v217, v209\n\t" \
  "v_mov_b32 v218, v210\n\t" \
  "v_mov_b32 v219, v211\n\t" \
  "v_mov_b32 v220, v212\n\t" \
  "v_mov_b32 v221, v213\n\t" \
  "v_mov_b32 v222, v214\n\t" \
  "v_mov_b32 v223, v215\n\t" \
  "v_permlane32_swap_b32 v208, v216\n\t" \
  "v_permlane32_swap_b32 v209, v217\n\t" \
  "v_permlane32_swap_b32 v210, v218\n\t" \
  "v_permlane32_swap_b32 v211, v219\n\t" \
  "v_permlane32_swap_b32 v212, v220\n\t" \
  "v_permlane32_swap_b32 v213, v221\n\t" \
  "v_permlane32_swap_b32 v214, v222\n\t" \
  "v_permlane32_swap_b32 v215, v223\n\t" \
  "v_add_f32 v208, v208, v216\n\t" \
  "v_add_f32 v209, v209, v217\n\t" \
  "v_add_f32 v210, v210, v218\n\t" \
  "v_add_f32 v211, v211, v219\n\t" \
  "v_add_f32 v212, v212, v220\n\t" \
  "v_add_f32 v213, v213, v221\n\t" \
  "v_add_f32 v214, v214, v222\n\t" \
  "v_add_f32 v215, v215, v223\n\t" \
  "v_cvt_pk_bf16_f32 v200, v208, v209\n\t" \
  "v_cvt_pk_bf16_f32 v201, v210, v211\n\t" \
  "v_cvt_pk_bf16_f32 v202, v212, v213\n\t" \
  "v_cvt_pk_bf16_f32 v203, v214, v215\n\t" \
  "v_cmp_gt_u32 vcc, 8, v32\n\t" \
  "s_nop 1\n\t" \
  "v_cndmask_b32 v200, v202, v200, vcc\n\t" \
  "v_cndmask_b32 v201, v203, v201, vcc\n\t" \
  "s_mov_b64 exec, 0xffff\n\t" \
  "buffer_store_dwordx2 v[200:201], v48, %[rO], %[t1] offen sc1\n\t" \
  "s_mov_b64 exec, -1\n\t" \
  ".Lw119_%=:\n\t" \
  "ds_read_b32 v53, v52\n\t" \
  "s_sub_i32 s61, s61, 1\n\t" \
  "s_cmp_gt_i32 s61, 0\n\t" \
  "s_waitcnt lgkmcnt(0)\n\t" \
  "v_cmp_eq_u32 vcc, 0, v53\n\t" \
  "s_cselect_b64 s[68:69], vcc, 0\n\t" \
  "s_cmp_lg_u64 s[68:69], 0\n\t" \
  "s_cbranch_scc1 .Lw119_%=\n\t" \
  "ds_read_b128 v[224:227], v51\n\t" \
  "s_lshl_b32 s75, %[bid], 11\n\t" \
  "s_lshl_b32 s76, s86, 9\n\t" \
  "s_add_u32 s75, s75, s76\n\t" \
  "s_add_u32 s75, s75, 135168\n\t" \
  "v_max_u32 v54, 8, v29\n\t" \
  "v_lshlrev_b32 v54, 4, v54\n\t" \
  "s_waitcnt lgkmcnt(0)\n\t" \
  "v_lshlrev_b32 v216, 16, v224\n\t" \
  "v_and_b32 v217, s64, v224\n\t" \
  "v_lshlrev_b32 v218, 16, v225\n\t" \
  "v_and_b32 v219, s64, v225\n\t" \
  "v_lshlrev_b32 v220, 16, v226\n\t" \
  "v_and_b32 v221, s64, v226\n\t" \
  "v_lshlrev_b32 v222, 16, v227\n\t" \
  "v_and_b32 v223, s64, v227\n\t" \
  "s_sleep 10\n\t" \
  ".Lw220_%=:\n\t" \
  "buffer_load_dwordx2 v[232:233], v54, %[rw], s75 offen sc1\n\t" \
  "s_sub_i32 s61, s61, 1\n\t" \
  "s_cmp_gt_i32 s61, 0\n\t" \
  "s_waitcnt vmcnt(0)\n\t" \
  "v_cmp_ne_u64 vcc, s[92:93], v[232:233]\n\t" \
  "s_cselect_b64 s[68:69], vcc, 0\n\t" \
  "s_cmp_lg_u64 s[68:69], 0\n\t" \
  "s_cbranch_scc1 .Lw220_%=\n\t" \
  "buffer_load_dwordx4 v[200:203], v30, %[rw], s74 offen sc1\n\t" \
  "buffer_load_dwordx4 v[204:207], v30, %[rw], s65 offen sc1\n\t" \
  "buffer_load_dwordx4 v[208:211], v30, %[rw], s66 offen sc1\n\t" \
  "s_waitcnt vmcnt(2)\n\t" \
  "v_lshlrev_b32 v224, 16, v200\n\t" \
  "v_and_b32 v225, s64, v200\n\t" \
  "v_lshlrev_b32 v226, 16, v201\n\t" \
  "v_and_b32 v227, s64, v201\n\t" \
  "v_lshlrev_b32 v228, 16, v202\n\t" \
  "v_and_b32 v229, s64, v202\n\t" \
  "v_lshlrev_b32 v230, 16, v203\n\t" \
  "v_and_b32 v231, s64, v203\n\t" \
  "v_pk_add_f32 v[216:217], v[216:217], v[224:225]\n\t" \
  "v_pk_add_f32 v[218:219], v[218:219], v[226:227]\n\t" \
  "v_pk_add_f32 v[220:221], v[220:221], v[228:229]\n\t" \
  "v_pk_add_f32 v[222:223], v[222:223], v[230:231]\n\t" \
  "s_waitcnt vmcnt(1)\n\t" \
  "v_lshlrev_b32 v224, 16, v204\n\t" \
  "v_and_b32 v225, s64, v204\n\t" \
  "v_lshlrev_b32 v226, 16, v205\n\t" \
  "v_and_b32 v227, s64, v205\n\t" \
  "v_lshlrev_b32 v228, 16, v206\n\t" \
  "v_and_b32 v229, s64, v206\n\t" \
  "v_lshlrev_b32 v230, 16, v207\n\t" \
  "v_and_b32 v231, s64, v207\n\t" \
  "v_pk_add_f32 v[216:217], v[216:217], v[224:225]\n\t" \
  "v_pk_add_f32 v[218:219], v[218:219], v[226:227]\n\t" \
  "v_pk_add_f32 v[220:221], v[220:221], v[228:229]\n\t" \
  "v_pk_add_f32 v[222:223], v[222:223], v[230:231]\n\t" \
  "s_waitcnt vmcnt(0)\n\t" \
  "v_lshlrev_b32 v224, 16, v208\n\t" \
  "v_and_b32 v225, s64, v208\n\t" \
  "v_lshlrev_b32 v226, 16, v209\n\t" \
  "v_and_b32 v227, s64, v209\n\t" \
  "v_lshlrev_b32 v228, 16, v210\n\t" \
  "v_and_b32 v229, s64, v210\n\t" \
  "v_lshlrev_b32 v230, 16, v211\n\t" \
  "v_and_b32 v231, s64, v211\n\t" \
  "v_pk_add_f32 v[216:217], v[216:217], v[224:225]\n\t" \
  "v_pk_add_f32 v[218:219], v[218:219], v[226:227]\n\t" \
  "v_pk_add_f32 v[220:221], v[220:221], v[228:229]\n\t" \
  "v_pk_add_f32 v[222:223], v[222:223], v[230:231]\n\t" \
  "v_cvt_pk_bf16_f32 v200, v216, v217\n\t" \
  "v_cvt_pk_bf16_f32 v201, v218, v219\n\t" \
  "v_cvt_pk_bf16_f32 v202, v220, v221\n\t" \
  "v_cvt_pk_bf16_f32 v203, v222, v223\n\t" \
  "s_nop 1\n\t" \
  "v_permlane16_swap_b32 v200, v202\n\t" \
  "v_permlane16_swap_b32 v201, v203\n\t" \
  "buffer_store_dwordx4 v[200:203], v49, %[rO], s59 offen sc1\n\t" \
  "s_branch .Lpg15_%=\n\t" \
  ".Lpb25_%=:\n\t" \
  "buffer_load_dwordx2 v[232:233], v29, %[rw], %[sf] offen offset:448 sc1\n\t" \
  "s_waitcnt vmcnt(0)\n\t" \
  "v_cmp_ne_u64 vcc, s[92:93], v[232:233]\n\t" \
  "s_cbranch_vccnz .Lrt24_%=\n\t" \
  "s_branch .Lgo22_%=\n\t" \
  ".Lph3_%=:\n\t" \
  "s_xor_b64 s[66:67], s[72:73], s[92:93]\n\t" \
  "s_xor_b64 s[64:65], s[74:75], s[92:93]\n\t" \
  "s_or_b64 s[66:67], s[66:67], s[64:65]\n\t" \
  "s_xor_b64 s[64:65], s[76:77], s[92:93]\n\t" \
  "s_or_b64 s[66:67], s[66:67], s[64:65]\n\t" \
  "s_xor_b64 s[64:65], s[78:79], s[92:93]\n\t" \
  "s_or_b64 s[66:67], s[66:67], s[64:65]\n\t" \
  "s_cbranch_scc1 .Lnx23_%=\n\t" \
  "s_cmp_eq_u32 s87, 0\n\t" \
  "s_cbranch_scc1 .Lrt24_%=\n\t" \
  ".Lgo22_%=:\n\t" \
  "s_add_u32 m0, s83, 0\n\t" \
  "s_nop 0\n\t" \
  "buffer_load_dwordx4 v21, %[rw], s80 offen offset:0 sc1 lds\n\t" \
  "buffer_load_dwordx4 v22, %[rw], s80 offen offset:1024 sc1 lds\n\t" \
  "s_add_u32 m0, s83, 2048\n\t" \
  "s_nop 0\n\t" \
  "buffer_load_dwordx4 v21, %[rw], s81 offen offset:0 sc1 lds\n\t" \
  "buffer_load_dwordx4 v22, %[rw], s81 offen offset:1024 sc1 lds\n\t" \
  "s_add_u32 m0, s83, 2048\n\t" \
  "s_nop 0\n\t" \
  "buffer_load_dwordx4 v21, %[rw], s80 offen offset:2048 sc1 lds\n\t" \
  "buffer_load_dwordx4 v22, %[rw], s80 offen offset:3072 sc1 lds\n\t" \
  "s_add_u32 m0, s83, 4096\n\t" \
  "s_nop 0\n\t" \
  "buffer_load_dwordx4 v21, %[rw], s81 offen offset:2048 sc1 lds\n\t" \
  "buffer_load_dwordx4 v22, %[rw], s81 offen offset:3072 sc1 lds\n\t" \
  "s_mov_b32 s62, -1\n\t" \
  "s_setpc_b64 s[88:89]\n\t" \
  ".Lnx23_%=:\n\t" \
  "s_cmp_ge_u32 s62, 256\n\t" \
  "s_cbranch_scc1 .Lpb25_%=\n\t" \
  "s_add_u32 s66, %[sf], s62\n\t" \
  "s_add_u32 s62, s62, 64\n\t" \
  "s_buffer_load_dwordx8 s[72:79], %[rw], s66\n\t" \
  ".Lrt24_%=:\n\t" \
  "s_setpc_b64 s[88:89]\n\t" \
  ".Lpg15_%=:\n\t" \
  "s_mov_b32 %[t0], s92\n\t" \
  "s_mov_b32 %[t1], s93\n\t"


typedef unsigned u32x2 __attribute__((ext_vector_type(2)));
typedef __amdgpu_buffer_rsrc_t rsrc_t;

constexpr int L_OWNB = 122880, L_OWN = 126976, L_TOTAL = 163840;
constexpr unsigned W_EPOCH = 0;
constexpr unsigned W_PFLAG = 4096;
constexpr unsigned W_FM    = W_PFLAG + 131072;
constexpr unsigned W_F8    = W_FM + 256 * 2048;
constexpr unsigned W_PBUF  = W_F8 + 32768;
constexpr unsigned W_INM   = W_PBUF + 1024 * 2048;
constexpr unsigned W_IN8   = W_INM + 256 * 12288;
constexpr unsigned W_END   = W_IN8 + 256 * 8192;

static __device__ __forceinline__ unsigned chunk_order(unsigned c) {
  const unsigned long long a = 0x876543210ull;
  const unsigned sh = 4 * (c + 1);
  return (unsigned)((a >> sh) | (a << (36 - sh)));
}
static __device__ __forceinline__ unsigned head_order(int mt, int nt) {
  const int m = mt & 3;
  const unsigned rel = m == 0 ? 0x67543210u : m == 1 ? 0x76452310u : m == 2 ? 0x75643210u : 0x76534210u;
  return (rel + 0x22222222u * (unsigned)nt) & 0x77777777u;
}

#define V10(b) "v" #b "0", "v" #b "1", "v" #b "2", "v" #b "3", "v" #b "4", "v" #b "5", "v" #b "6", "v" #b "7", "v" #b "8", "v" #b "9"
#define V0_9 "v0", "v1", "v2", "v3", "v4", "v5", "v6", "v7", "v8", "v9"
#define LOAD_S "s82", "s83", "s84", "s85", "s86", "s87", "s88", "s89", "s90", "s91", "s92", "s93"
#define ALL_V V0_9, V10(1), V10(2), V10(3), V10(4), V10(5), V10(6), V10(7), V10(8), V10(9), V10(10), V10(11), V10(12), V10(13), V10(14), V10(15), V10(16), \
              V10(17), V10(18), V10(19), V10(20), V10(21), V10(22), V10(23), V10(24), "v250", "v251", "v252", "v253", "v254", "v255"

extern "C" __global__ __launch_bounds__(512, 2) void fused_attn_blocks(const char* xA, const char* kA, const char* vA, const char* wqA, const char* woA,
                                                                      const char* xB, const char* kB, const char* vB, const char* wqB, const char* woB,
                                                                      char* out, char* ws) {
  unsigned z;
  asm volatile(LOAD_ASM_0 : [z] "=s"(z) : [ka] "s"(__builtin_amdgcn_kernarg_segment_ptr()), [bid] "s"(blockIdx.x), [tid] "v"(threadIdx.x)
               : "memory", "scc", "v251", "v252", "s76", "s77", LOAD_S);
  const int bid = blockIdx.x + z, xcd = bid & 7, idx = bid >> 3;
  const int br = xcd & 1, ks = xcd >> 1, hl = idx & 7, mg = idx >> 3, mt = idx >> 2, nt = idx & 3;
  const int wave = __builtin_amdgcn_readfirstlane(threadIdx.x >> 6);
  const int lw = wave - 4, head = ks * 8 + hl;
  const unsigned c0 = (hl + 2 * mg) % 9, sx0 = (mg * 128 + lw * 32) * 1152, la = lw * 4096, sq0 = lw * 16 * 8192 + head * 256;
  asm volatile(LOAD_ASM_A : [z] "=s"(z) : [wave] "s"(wave), [la] "s"(la), [sx] "s"(sx0 + c0 * 128), [sq] "s"(sq0 + (c0 << 19))
               : "memory", "scc", "v251", "v252", "s76", "s77", "s78", "s79", "v0", "v1", "v2", "v3", "v4", "v5", "v6", "v7", "v20", V10(3), V10(4), LOAD_S);
  ws += z;
  const rsrc_t rW = __builtin_amdgcn_make_buffer_rsrc(ws, 0, W_END, 0x20000);

  if (__builtin_expect(wave >= 4, 1)) {
    const rsrc_t rX  = __builtin_amdgcn_make_buffer_rsrc((void*)((br ? xB : xA) + z), 0, 512 * 1152, 0x20000);
    const rsrc_t rWq = __builtin_amdgcn_make_buffer_rsrc((void*)((br ? wqB : wqA) + z), 0, 576 * 8192, 0x20000);
    const int hkv = br ? 32 : 16, sh = 1 - br + z, mtz = mt + z, lwz = lw + z, rev = mtz >> 2;
    const rsrc_t rK  = __builtin_amdgcn_make_buffer_rsrc((void*)((br ? kB : kA) + z), 0, 16 * hkv * 8192, 0x20000);
    const rsrc_t rV  = __builtin_amdgcn_make_buffer_rsrc((void*)((br ? vB : vA) + z), 0, 16 * hkv * 8192, 0x20000);
    const rsrc_t rWo = __builtin_amdgcn_make_buffer_rsrc((void*)((br ? woB : woA) + z), 0, 4096 * 1152, 0x20000);
    const rsrc_t rO  = __builtin_amdgcn_make_buffer_rsrc(out + z, 0, 512 * 2304, 0x20000);
    unsigned ord = chunk_order(c0 + z), t0 = W_EPOCH + (br * 4 + mg) * 128, t1, sk = ((mg * 4 + lwz) * hkv + (head >> sh)) * 8192;
    asm volatile(LOAD_ASM_B
                 : [t0] "+s"(t0), [t1] "=&s"(t1), [ord] "+s"(ord), [sk] "+s"(sk)
                 : [rx] "s"(rX), [rq] "s"(rWq), [rk] "s"(rK), [rv] "s"(rV), [ro] "s"(rWo), [rw] "s"(rW), [rO] "s"(rO), [la] "s"(la), [sq] "s"(sq0), [sx] "s"(sx0),
                   [nt] "s"(nt), [sh] "s"(sh), [or2] "s"(head_order(mtz, nt)), [kp] "s"(2 + (hl + 3 * mg + z) % 6), [bid] "s"(bid), [dw] "s"(rev ? -64 * 1152 : 64 * 1152),
                   [sv] "s"(((2 * mtz + ((lwz ^ ks) >> 1)) * hkv + ((ks * 8) >> sh)) * 8192 + (lwz & 1) * 4096 + rev * 128),
                   [so] "s"((ks * 1024 + lwz * 16 + rev * 64) * 1152 + nt * 256),
                   [sp] "s"(W_PBUF + (unsigned)((br * 16 + 2 * mtz) * 32 + ks * 8 + 2 * lwz) * 2048), [sf] "s"(W_PFLAG + ((br * 8 + mtz) * 16 + ks * 4 + lwz) * 512),
                   [oo] "s"(((2 * mtz + (ks >> 1)) * 32 + (ks & 1) * 16) * 2304 + (br * 576 + nt * 128) * 2)
                 : "memory", "vcc", "scc", V0_9, V10(1), V10(2), V10(3), V10(4), V10(5), V10(20), V10(21), V10(22), "v230", "v231", "v232", "v233", LOAD_S,
                   "s58", "s59", "s60", "s61", "s62", "s64", "s65", "s66", "s67", "s68", "s69", "s70", "s71", "s72", "s73", "s74", "s75", "s76", "s77", "s78", "s79", "s80", "s81");
    if (lw == 0 && ks == 0 && hl == 0 && __builtin_amdgcn_mbcnt_hi(-1, __builtin_amdgcn_mbcnt_lo(-1, 0)) == 0) {
      const u32x2 t = {t0, t1};
      __builtin_amdgcn_raw_buffer_store_b64(t, rW, W_EPOCH + (br * 4 + mg) * 128, 0, 16);
    }
    return;
  }

  const int w = wave;
  const unsigned pown = (unsigned)((br * 16 + mg * 4 + w) * 32 + ks * 8 + hl), src = ks * 4 + nt, ob8 = (mt * 4 + w) * 8 + br;
  unsigned t0, t1, t2, tlo, thi;
  asm volatile(CONS_ASM ".Lfab_end:"
               : [t0] "=&s"(t0), [t1] "=&s"(t1), [t2] "=&s"(t2), [tlo] "=&s"(tlo), [thi] "=&s"(thi)
               : [s8] "s"(W_IN8 + ob8 * 8192 + src * 512), [sm] "s"(W_INM + (idx * 8 + br) * 12288 + w * 1024), [cpf] "s"(4096 + (idx * 4 + w) * 128),
                 [lownb] "s"(L_OWNB + w * 1024), [w] "s"(w), [ks] "s"(ks), [rw] "s"(rW), [lown] "s"(L_OWN + 4 * w), [or2] "s"(head_order(mt, nt)),
                 [sfm] "s"(W_FM + (idx * 8 + br) * 2048 + w * 512), [spown] "s"(W_PBUF + pown * 2048),
                 [spflag] "s"(W_PFLAG + ((br * 8 + mg * 2 + (w >> 1)) * 16 + ks * 4 + (hl >> 1)) * 512 + ((w & 1) * 2 + (hl & 1)) * 8),
                 [sep] "s"(W_EPOCH + (br * 4 + mg) * 128), [sf8] "s"(W_F8 + ob8 * 128 + src * 8)
               : "memory", "vcc", "scc", "s90", "s91", ALL_V);
}

extern "C" void kernel_launch(void* const* d_in, const int* in_sizes, int n_in, void* d_out, int out_size, void* d_ws, size_t ws_size, hipStream_t stream) {
  (void)in_sizes; (void)n_in; (void)out_size; (void)ws_size;
  (void)hipFuncSetAttribute((const void*)fused_attn_blocks, hipFuncAttributeMaxDynamicSharedMemorySize, L_TOTAL);
  hipLaunchKernelGGL(fused_attn_blocks, dim3(256), dim3(512), L_TOTAL, stream,
                     (const char*)d_in[0], (const char*)d_in[1], (const char*)d_in[2], (const char*)d_in[3], (const char*)d_in[4],
                     (const char*)d_in[5], (const char*)d_in[6], (const char*)d_in[7], (const char*)d_in[8], (const char*)d_in[9],
                     (char*)d_out, (char*)d_ws);
}
```
